```python
import torch

def _softmax(x, axis=-1):
    return torch.softmax(x, dim=axis)
def _gelu(x):
    return torch.nn.functional.gelu(x, approximate="tanh")
def _silu(x):
    return torch.nn.functional.silu(x)
def _sigmoid(x):
    return torch.sigmoid(x)
def _layernorm(x, axis=-1, eps=1e-5):
    m = x.mean(dim=axis, keepdim=True)
    v = x.var(dim=axis, keepdim=True, unbiased=False)
    return (x - m) / torch.sqrt(v + eps)
def _scatter_add(tgt, idx, src, axis=0):
    return tgt.clone().index_add_(axis, idx, src)
def _segment_sum(src, seg, n):
    out = torch.zeros((n,) + src.shape[1:], dtype=src.dtype)
    return out.index_add_(0, seg, src)
def _pad(x, pw):
    flat = tuple(v for pair in reversed(pw) for v in pair)
    return torch.nn.functional.pad(x, flat)

kernel_name = 'concat_heads__sliding_local_dilated__linear_attn_block__prompt'

def setup_inputs():

    _w = (lambda a: (lambda t: t.to(torch.bfloat16) if t.is_floating_point() else t)(torch.as_tensor(a)))
    def _A_setup():
        B, S, D = 1, 2048, 640
        return {
            "x": _w((lambda *s: torch.randn(*s, dtype=torch.bfloat16) * 0.2)(B, S, D)),
            "wqkv": _w((lambda *s: torch.randn(*s, dtype=torch.bfloat16) * 0.2)(D, 3 * D)),
            "wo": _w((lambda *s: torch.randn(*s, dtype=torch.bfloat16) * 0.05)(D, D)),
        }
    _A_in = _A_setup()

    def _B_setup():
        B, S, D = 1, 2048, 640
        return {
            "x": _w((lambda *s: torch.randn(*s, dtype=torch.bfloat16) * 0.05)(B, S, D)),
            "wqkv": _w((lambda *s: torch.randn(*s, dtype=torch.bfloat16) * 1.6)(D, 3 * D)),
            "wo": _w((lambda *s: torch.randn(*s, dtype=torch.bfloat16) * 0.8)(D, D)),
        }
    _B_in = _B_setup()

    return {
        "A_x": _A_in["x"],
        "A_wqkv": _A_in["wqkv"],
        "A_wo": _A_in["wo"],
        "B_x": _B_in["x"],
        "B_wqkv": _B_in["wqkv"],
        "B_wo": _B_in["wo"],
    }

def reference(A_x, A_wqkv, A_wo, B_x, B_wqkv, B_wo):
    def _A_ref(x, wqkv, wo):
        B, S, D = x.shape
        H = 32; Dh = D // H
        W, dil = 8, 2
        qkv = (lambda x, *d: x.permute(*d))((x @ wqkv).reshape(B, S, 3, H, Dh), 2, 0, 3, 1, 4)
        q, k, v = qkv[0], qkv[1], qkv[2]
        scale = Dh ** -0.5
        pos = torch.arange(S)
        off = torch.arange(W) * dil
        idx = pos[:, None] - off[None, :]
        mask = idx >= 0
        idx_c = torch.where(mask, idx, 0).reshape(-1)
        kg = k[:, :, idx_c, :].reshape(B, H, S, W, Dh)
        vg = v[:, :, idx_c, :].reshape(B, H, S, W, Dh)
        logits = torch.einsum("bhsd,bhswd->bhsw", q, kg) * scale
        logits = torch.where(mask[None, None, :, :], logits, -1e9)
        attn = _softmax(logits, axis=-1)
        o = torch.einsum("bhsw,bhswd->bhsd", attn, vg)
        return (lambda x, *d: x.permute(*d))(o, 0, 2, 1, 3).reshape(B, S, D) @ wo

    def _B_ref(x, wqkv, wo):
        B, S, D = x.shape
        H = 32; Dh = D // H
        qkv = (lambda x, *d: x.permute(*d))((x @ wqkv).reshape(B, S, 3, H, Dh), 2, 0, 3, 1, 4)
        q, k, v = qkv[0], qkv[1], qkv[2]
        phi = lambda t: torch.where(t > 0, t + 1, torch.exp(t)) + 1e-6
        pq, pk = phi(q), phi(k)
        kv = torch.einsum("bhsd,bhse->bhde", pk, v)
        z = torch.einsum("bhsd->bhd", pk)
        num = torch.einsum("bhqd,bhde->bhqe", pq, kv)
        den = torch.einsum("bhqd,bhd->bhq", pq, z)[..., None] + 1e-6
        o = num / den
        return (lambda x, *d: x.permute(*d))(o, 0, 2, 1, 3).reshape(B, S, D) @ wo

    a = _A_ref(A_x, A_wqkv, A_wo)
    b = _B_ref(B_x, B_wqkv, B_wo)
    return torch.concatenate([a, b], axis=-1)
```

```cpp
#include <hip/hip_runtime.h>
#include <hip/hip_bf16.h>
#include <stdint.h>

typedef unsigned short u16;
static __device__ __forceinline__ float bf2f(u16 v) { return __uint_as_float(((unsigned)v) << 16); }
static __device__ __forceinline__ u16 f2bf(float f) { __hip_bfloat16 b = __float2bfloat16(f); return *reinterpret_cast<u16*>(&b); }
static __device__ __forceinline__ float rbf(float f) { return bf2f(f2bf(f)); }

constexpr int S = 2048, D = 640, H = 32, DH = 20;

__global__ void gemm_naive(const u16* A, const u16* B, u16* C, int N, int K, int ldc) {
    __shared__ float sA[16][17], sB[16][17];
    int tx = threadIdx.x, ty = threadIdx.y;
    int row = blockIdx.y * 16 + ty, col = blockIdx.x * 16 + tx;
    float acc = 0.f;
    for (int k0 = 0; k0 < K; k0 += 16) {
        sA[ty][tx] = bf2f(A[(size_t)row * K + k0 + tx]);
        sB[ty][tx] = bf2f(B[(size_t)(k0 + ty) * N + col]);
        __syncthreads();
        for (int k = 0; k < 16; ++k) acc += sA[ty][k] * sB[k][tx];
        __syncthreads();
    }
    C[(size_t)row * ldc + col] = f2bf(acc);
}

__global__ void attnA(const u16* qkv, u16* o) {
    int idx = blockIdx.x * blockDim.x + threadIdx.x;
    if (idx >= S * H) return;
    int s = idx / H, h = idx % H;
    const float scale = 0.22360679774997896f;
    float q[DH];
    for (int d = 0; d < DH; ++d) q[d] = bf2f(qkv[(size_t)s * 3 * D + h * DH + d]);
    float lg[8]; float m = -3e38f;
    for (int w = 0; w < 8; ++w) {
        int t = s - 2 * w;
        if (t >= 0) {
            float a = 0.f;
            for (int d = 0; d < DH; ++d) a += q[d] * bf2f(qkv[(size_t)t * 3 * D + D + h * DH + d]);
            lg[w] = rbf(rbf(a) * scale);
        } else lg[w] = rbf(-1e9f);
        m = fmaxf(m, lg[w]);
    }
    float sum = 0.f;
    for (int w = 0; w < 8; ++w) { lg[w] = __expf(lg[w] - m); sum += lg[w]; }
    for (int w = 0; w < 8; ++w) lg[w] = rbf(lg[w] / sum);
    for (int d = 0; d < DH; ++d) {
        float a = 0.f;
        for (int w = 0; w < 8; ++w) {
            int t = s - 2 * w; if (t < 0) t = 0;
            a += lg[w] * bf2f(qkv[(size_t)t * 3 * D + 2 * D + h * DH + d]);
        }
        o[(size_t)s * D + h * DH + d] = f2bf(a);
    }
}

static __device__ __forceinline__ float phi(float t) {
    float r = t > 0.f ? rbf(t + 1.f) : rbf(__expf(t));
    return rbf(r + 1e-6f);
}

__global__ void kvB(const u16* qkv, float* kv) {
    int h = blockIdx.x; int t = threadIdx.x;
    if (t >= DH * 21) return;
    int d = t / 21, e = t % 21;
    float a = 0.f;
    for (int s = 0; s < S; ++s) {
        float pk = phi(bf2f(qkv[(size_t)s * 3 * D + D + h * DH + d]));
        float v = e < 20 ? bf2f(qkv[(size_t)s * 3 * D + 2 * D + h * DH + e]) : 1.f;
        a += pk * v;
    }
    kv[h * 420 + t] = rbf(a);
}

__global__ void oB(const u16* qkv, const float* kv, u16* o) {
    int idx = blockIdx.x * blockDim.x + threadIdx.x;
    if (idx >= S * H) return;
    int s = idx / H, h = idx % H;
    float pq[DH];
    for (int d = 0; d < DH; ++d) pq[d] = phi(bf2f(qkv[(size_t)s * 3 * D + h * DH + d]));
    float den = 0.f;
    for (int d = 0; d < DH; ++d) den += pq[d] * kv[h * 420 + d * 21 + 20];
    den = rbf(rbf(den) + 1e-6f);
    for (int e = 0; e < DH; ++e) {
        float a = 0.f;
        for (int d = 0; d < DH; ++d) a += pq[d] * kv[h * 420 + d * 21 + e];
        o[(size_t)s * D + h * DH + e] = f2bf(rbf(a) / den);
    }
}

extern "C" void kernel_launch(void* const* d_in, const int* in_sizes, int n_in,
                              void* d_out, int out_size, void* d_ws, size_t ws_size,
                              hipStream_t stream) {
    const u16* Ax = (const u16*)d_in[0];
    const u16* Aw = (const u16*)d_in[1];
    const u16* Ao = (const u16*)d_in[2];
    const u16* Bx = (const u16*)d_in[3];
    const u16* Bw = (const u16*)d_in[4];
    const u16* Bo = (const u16*)d_in[5];
    u16* out = (u16*)d_out;
    char* ws = (char*)d_ws;
    u16* qkvA = (u16*)ws; ws += (size_t)S * 3 * D * 2;
    u16* qkvB = (u16*)ws; ws += (size_t)S * 3 * D * 2;
    u16* oA = (u16*)ws; ws += (size_t)S * D * 2;
    u16* oBb = (u16*)ws; ws += (size_t)S * D * 2;
    float* kv = (float*)ws;
    dim3 b(16, 16);
    gemm_naive<<<dim3(3 * D / 16, S / 16), b, 0, stream>>>(Ax, Aw, qkvA, 3 * D, D, 3 * D);
    gemm_naive<<<dim3(3 * D / 16, S / 16), b, 0, stream>>>(Bx, Bw, qkvB, 3 * D, D, 3 * D);
    attnA<<<S * H / 256, 256, 0, stream>>>(qkvA, oA);
    kvB<<<H, 448, 0, stream>>>(qkvB, kv);
    oB<<<S * H / 256, 256, 0, stream>>>(qkvB, kv, oBb);
    gemm_naive<<<dim3(D / 16, S / 16), b, 0, stream>>>(oA, Ao, out, D, D, 2 * D);
    gemm_naive<<<dim3(D / 16, S / 16), b, 0, stream>>>(oBb, Bo, out + D, D, D, 2 * D);
}
```

```cpp
#include <hip/hip_runtime.h>
#include <stdint.h>

typedef unsigned short u16;
typedef unsigned int u32;
typedef unsigned long long u64;
typedef __attribute__((ext_vector_type(4))) short s16x4;
typedef __attribute__((ext_vector_type(8))) __bf16 bf16x8;
typedef __attribute__((ext_vector_type(2))) __bf16 bf16x2;
typedef __attribute__((ext_vector_type(4))) float f32x4;
typedef __attribute__((ext_vector_type(2))) float f32x2;
typedef __attribute__((ext_vector_type(4))) u32 u32x4;
typedef __attribute__((ext_vector_type(2))) u32 u32x2;
typedef __amdgpu_buffer_rsrc_t rsrc_t;
#define LAS __attribute__((address_space(3)))
#define GAS __attribute__((address_space(1)))
#define DI __device__ __forceinline__

#define XMAP 2

constexpr int SEQ = 2048, DM = 640;
constexpr int NSLOT = 6, SLOT_B = 20480;
constexpr int RING_B = NSLOT * SLOT_B;
constexpr int XCH_OFF = RING_B;
constexpr int XCH_B = 32768;
constexpr int LDS_B = RING_B + XCH_B;

constexpr u32 WS_EPOCH = 0;
constexpr u32 WS_F1 = 4096;
constexpr u32 WS_F2 = WS_F1 + 256 * 4 * 128;
constexpr u32 WS_P1 = WS_F2 + 256 * 4 * 128;
constexpr u32 WS_O = WS_P1 + 256 * 10240;
constexpr u32 WS_TS = WS_O + 2 * 16 * 4 * 2 * 8 * 2560;
constexpr u32 WS_BYTES = WS_TS + 256 * 4 * 16 * 8;

constexpr int NST = 20;
__host__ __device__ constexpr int st_type(int s) { constexpr int T[NST] = {1,1,2,1,2,1,2,1,2,2,0,0,0,0,0,3,3,3,3,3}; return T[s]; }
__host__ __device__ constexpr int st_chunk(int s) { constexpr int C[NST] = {0,1,0,2,1,3,2,4,3,4,0,1,2,3,4,0,1,2,3,4}; return C[s]; }

struct Sched { int n_init; int n_sync[NST]; };
__host__ __device__ constexpr Sched make_sched() {
    Sched r{};
    int endW[NST] = {}, endX[5] = {};
    int pos = 0;
    pos += 5; endW[0] = pos; pos += 8; endX[0] = pos; pos += 2;
    pos += 5; endW[1] = pos; pos += 8; endX[1] = pos;
    pos += 5; endW[2] = pos; pos += 5; endW[3] = pos; pos += 8; endX[2] = pos;
    pos += 5; endW[4] = pos; pos += 5; endW[5] = pos;
    r.n_init = pos - endX[0];
    for (int s = 0; s + 1 < NST; ++s) {
        int need = endW[s + 1];
        if (st_type(s + 1) == 1 && endX[st_chunk(s + 1)] > need) need = endX[st_chunk(s + 1)];
        r.n_sync[s] = pos - need;
        if (s == 0) { pos += 8; endX[3] = pos; }
        if (s == 1) { pos += 8; endX[4] = pos; }
        if (s + NSLOT < NST) { pos += 5; endW[s + NSLOT] = pos; }
    }
    return r;
}
constexpr Sched SCHED = make_sched();

template <int V> struct IC { static constexpr int v = V; };
template <int I, int N, class F> DI void sfor(F&& f) { if constexpr (I < N) { f(IC<I>{}); sfor<I + 1, N>(f); } }

DI f32x4 mma(u32x4 a, u32x4 b, f32x4 c) {
    return __builtin_amdgcn_mfma_f32_16x16x32_bf16(__builtin_bit_cast(bf16x8, a), __builtin_bit_cast(bf16x8, b), c, 0, 0, 0);
}
DI u32 pk2(float a, float b) { f32x2 v = {a, b}; return __builtin_bit_cast(u32, __builtin_convertvector(v, bf16x2)); }
DI u32x2 pk4(f32x4 v) { u32x2 r = {pk2(v[0], v[1]), pk2(v[2], v[3])}; return r; }
DI u32x4 cat(u32x2 a, u32x2 b) { u32x4 r = {a[0], a[1], b[0], b[1]}; return r; }
DI u32x2 trread(u32 addr) { return __builtin_bit_cast(u32x2, __builtin_amdgcn_ds_read_tr16_b64_v4i16((LAS s16x4*)addr)); }
DI float vmax(float a, float b) { float r; asm("v_max_f32 %0, %1, %2" : "=v"(r) : "v"(a), "v"(b)); return r; }
DI float vmax3(float a, float b, float c) { float r; asm("v_max3_f32 %0, %1, %2, %3" : "=v"(r) : "v"(a), "v"(b), "v"(c)); return r; }
DI float xmax(float m) {
    u32 a = __float_as_uint(m);
    auto r = __builtin_amdgcn_permlane16_swap(a, a, false, false);
    m = vmax(__uint_as_float(r[0]), __uint_as_float(r[1]));
    a = __float_as_uint(m);
    auto r2 = __builtin_amdgcn_permlane32_swap(a, a, false, false);
    return vmax(__uint_as_float(r2[0]), __uint_as_float(r2[1]));
}
DI rsrc_t mk_rsrc(const void* p, u32 bytes) { return __builtin_amdgcn_make_buffer_rsrc((void*)p, (short)0, (int)bytes, 0x00020000); }
DI u64 mix(u64 e) { return e * 0x9E3779B97F4A7C15ull + 0x632BE59BD9B4E019ull; }

template <int OFF> DI void ld128(u32x4& d, u32 voff, rsrc_t rs) {
    asm volatile("buffer_load_dwordx4 %0, %1, %2, 0 offen offset:%3" : "=v"(d) : "v"(voff), "s"(rs), "i"(OFF));
}
template <int OFF> DI void ld128_sc1(u32x4& d, u32 voff, rsrc_t rs, u32 soff) {
    asm volatile("buffer_load_dwordx4 %0, %1, %2, %3 offen offset:%4 sc1" : "=v"(d) : "v"(voff), "s"(rs), "s"(soff), "i"(OFF));
}
template <int OFF> DI void ld64_sc1(u32x2& d, u32 voff, rsrc_t rs, u32 soff) {
    asm volatile("buffer_load_dwordx2 %0, %1, %2, %3 offen offset:%4 sc1" : "=v"(d) : "v"(voff), "s"(rs), "s"(soff), "i"(OFF));
}
template <int LDSOFF> DI void glds(u32 ldsw, u32 voff, rsrc_t rs, u32 soff) {
    asm volatile("s_add_u32 m0, %0, %4\n\ts_nop 0\n\tbuffer_load_dwordx4 %1, %2, %3 offen lds"
                 :: "s"(ldsw), "v"(voff), "s"(rs), "s"(soff), "i"(LDSOFF) : "scc");
}
template <int N> DI void wait_vm() { asm volatile("s_waitcnt vmcnt(%0)" :: "i"(N) : "memory"); }
DI void pin(u32x4& a) { asm volatile("" : "+v"(a)); }
DI void pin(u32x2& a) { asm volatile("" : "+v"(a)); }

DI void poll(rsrc_t ws, u32 voff, u64 tag) {
    for (int it = 0; it < (1 << 18); ++it) {
        u32x2 v = __builtin_amdgcn_raw_buffer_load_b64(ws, voff, 0, 16);
        bool ok = (v[0] == (u32)tag) & (v[1] == (u32)(tag >> 32));
        if (__builtin_amdgcn_ballot_w64(!ok) == 0) break;
        __builtin_amdgcn_s_sleep(1);
    }
    asm volatile("" ::: "memory");
}
DI void set_flag(rsrc_t ws, u32 off, u64 tag, int lane) {
    if (lane == 0) { u32x2 v = {(u32)tag, (u32)(tag >> 32)}; __builtin_amdgcn_raw_buffer_store_b64(v, ws, off, 0, 16); }
}

#define TS(i) do {} while (0)

DI float phi(float t) {
    float e;
    float y = t * 1.4426950408889634f;
    asm("v_exp_f32 %0, %1 clamp" : "=v"(e) : "v"(y));
    return vmax(t + 1.0f, e) + 1e-6f;
}
DI float rbf(float x) { return __uint_as_float(pk2(x, 0.f) << 16); }

template <bool ISA>
DI void body(const u16* __restrict__ xg, const u16* __restrict__ wg_, const u16* __restrict__ wog, u16* __restrict__ outg,
             char* wsp, int t, int g, char* smem) {
    const int tid = threadIdx.x;
    const int lane = tid & 63;
    const int w = __builtin_amdgcn_readfirstlane(tid >> 6);
    const int gam = lane >> 4, c16 = lane & 15;
    const int b = ISA ? 0 : 1;
    const int wg = (b * 16 + t) * 8 + g;
    (void)wg;
    const u32 lds0 = (u32)(size_t)(LAS char*)smem;

    const rsrc_t rsW = mk_rsrc(wg_, DM * 3 * DM * 2);
    const rsrc_t rsX = mk_rsrc(xg + (size_t)(t * 128 + w * 32) * DM, 32 * DM * 2);
    const rsrc_t rsWo = mk_rsrc(wog, DM * DM * 2);
    const rsrc_t rsWS = mk_rsrc(wsp, WS_BYTES);
    TS(0);

    u32 vw[5];
    {
#pragma unroll
        for (int i = 0; i < 5; ++i) {
            int c = i * 64 + lane;
            int rl = c / 10, ch = c - rl * 10;
            int k = w * 32 + (rl ^ ((rl >> 1) & 4));
            vw[i] = (u32)(k * 3840 + ch * 16);
        }
    }
    const u32 ldsw = lds0 + (u32)w * 5120u;
    const u32 vx = (u32)(c16 * 2560 + gam * 16);
    u32x4 xf[2][20];

    auto issueW = [&](auto S) {
        constexpr int s = S.v;
        constexpr int ty = st_type(s), ck = st_chunk(s);
        const u32 soff = (u32)(ck * 128 * 3840 + ty * 1280) + (u32)g * 160u;
        sfor<0, 5>([&](auto I) { glds<(s % NSLOT) * SLOT_B + I.v * 1024>(ldsw, vw[I.v], rsW, soff); });
    };
    auto issueX = [&](auto C) {
        constexpr int c = C.v;
        sfor<0, 2>([&](auto P) {
            sfor<0, 4>([&](auto K) { ld128<P.v * 1280 + (c * 4 + K.v) * 64>(xf[P.v][c * 4 + K.v], vx, rsX); });
        });
    };
    u32x2 ep0, ep1;
    issueW(IC<0>{}); issueX(IC<0>{});
    {
        u32 o0 = ISA ? (u32)(128 * (1 + t)) : 0u;
        u32 o1 = ISA ? (u32)(128 * t) : 0u;
        u32 z = 0;
        ld64_sc1<0>(ep0, z, rsWS, o0);
        ld64_sc1<0>(ep1, z, rsWS, o1);
    }
    issueW(IC<1>{}); issueX(IC<1>{}); issueW(IC<2>{}); issueW(IC<3>{}); issueX(IC<2>{}); issueW(IC<4>{}); issueW(IC<5>{});

    u32 vo[5], vo4[5];
    {
#pragma unroll
        for (int i = 0; i < 5; ++i) {
            int c = i * 64 + lane;
            int rl = c / 10, ch = c - rl * 10;
            int k = rl ^ ((rl >> 1) & 4);
            int ga = k >> 3, e = k & 7;
            int row = 20 * (2 * w + (e >> 2)) + 4 * ga + (e & 3);
            int row4 = 20 * (8 * w + 4 * (e >> 2) + ga) + 16 + (e & 3);
            vo[i] = (u32)(row * 1280 + ch * 16);
            vo4[i] = (u32)(row4 * 1280 + ch * 16);
        }
    }
    auto issueO = [&](auto S) {
        constexpr int s = S.v;
        constexpr int ck = st_chunk(s);
        const u32 soff = (u32)(ck < 4 ? ck * 160 * 1280 : 0) + (u32)g * 160u;
        sfor<0, 5>([&](auto I) { glds<(s % NSLOT) * SLOT_B + I.v * 1024>(ldsw, ck < 4 ? vo[I.v] : vo4[I.v], rsWo, soff); });
    };

    u32 rb[2][2][2];
    {
        int q = (lane >> 2) & 3, p = lane & 3;
#pragma unroll
        for (int tt = 0; tt < 2; ++tt) {
            int row = 8 * gam + 4 * (tt ^ (gam & 1)) + q;
            rb[0][tt][0] = lds0 + (u32)(row * 160 + 8 * p);
            rb[0][tt][1] = lds0 + (u32)(row * 160 + 40 * p + 32);
            rb[1][tt][0] = rb[0][tt][0] + 3 * SLOT_B;
            rb[1][tt][1] = rb[0][tt][1] + 3 * SLOT_B;
            asm volatile("" : "+v"(rb[0][tt][0]), "+v"(rb[0][tt][1]), "+v"(rb[1][tt][0]), "+v"(rb[1][tt][1]));
        }
    }
    auto readB = [&](auto N, u32x4 (&B)[5]) {
        constexpr int n = N.v, s = n >> 2, kk = n & 3, slot = s % NSLOT, hf = slot / 3;
        constexpr int off = (slot % 3) * SLOT_B + kk * 5120;
#pragma unroll
        for (int f = 0; f < 4; ++f) B[f] = cat(trread(rb[hf][0][0] + off + 40 * f), trread(rb[hf][1][0] + off + 40 * f));
        B[4] = cat(trread(rb[hf][0][1] + off), trread(rb[hf][1][1] + off));
    };

    f32x4 acc[3][2][5];
#pragma unroll
    for (int a = 0; a < 3; ++a)
#pragma unroll
        for (int p = 0; p < 2; ++p)
#pragma unroll
            for (int f = 0; f < 5; ++f) acc[a][p][f] = (f32x4){0.f, 0.f, 0.f, 0.f};

    u32x4 Bf[2][5];
    auto pinX = [&](auto C) { sfor<0, 2>([&](auto P) { sfor<0, 4>([&](auto K) { pin(xf[P.v][C.v * 4 + K.v]); }); }); };

    wait_vm<SCHED.n_init>();
    asm volatile("s_barrier" ::: "memory");
    pinX(IC<0>{});
    TS(1);
    readB(IC<0>{}, Bf[0]);

    auto sync = [&](auto S) {
        constexpr int s = S.v;
        asm volatile("s_waitcnt vmcnt(%0) lgkmcnt(0)\n\ts_barrier" :: "i"(SCHED.n_sync[s]) : "memory");
        if constexpr (st_type(s + 1) == 1) pinX(IC<st_chunk(s + 1)>{});
        if constexpr (s == 0) issueX(IC<3>{});
        if constexpr (s == 1) issueX(IC<4>{});
        if constexpr (s + NSLOT < NST) {
            if constexpr (st_type(s + NSLOT) == 3) issueO(IC<s + NSLOT>{}); else issueW(IC<s + NSLOT>{});
        }
    };
    auto step = [&](auto N) {
        constexpr int n = N.v, s = n >> 2, kk = n & 3, ty = st_type(s), ck = st_chunk(s);
        constexpr int ai = ty == 3 ? 0 : ty;
        constexpr bool trans = (ty == 0 || ty == 3) || (ISA && ty == 1);
        if constexpr (kk == 3 && s + 1 < NST) sync(IC<s>{});
        if constexpr (n + 1 < NST * 4) readB(IC<n + 1>{}, Bf[(n + 1) & 1]);
#pragma unroll
        for (int f = 0; f < 5; ++f)
#pragma unroll
            for (int p = 0; p < 2; ++p) {
                if constexpr (trans) acc[ai][p][f] = mma(Bf[n & 1][f], xf[p][ck * 4 + kk], acc[ai][p][f]);
                else                 acc[ai][p][f] = mma(xf[p][ck * 4 + kk], Bf[n & 1][f], acc[ai][p][f]);
            }
    };

    sfor<0, 40>(step);
    TS(2);
    pin(ep0); pin(ep1);
    const u64 tag = mix(((u64)(u32)__builtin_amdgcn_readfirstlane(ep0[1]) << 32) | (u32)__builtin_amdgcn_readfirstlane(ep0[0]));
    const u64 tagp = mix(((u64)(u32)__builtin_amdgcn_readfirstlane(ep1[1]) << 32) | (u32)__builtin_amdgcn_readfirstlane(ep1[0]));
    const u32 myF1 = WS_F1 + (u32)(wg * 4 + w) * 128u;
    const u32 myF2 = WS_F2 + (u32)(wg * 4 + w) * 128u;
    const u32 xch = lds0 + XCH_OFF;

    u32x2 kp[2][5], vp[2][5];
    if constexpr (ISA) {
#pragma unroll
        for (int p = 0; p < 2; ++p)
#pragma unroll
            for (int f = 0; f < 5; ++f) { kp[p][f] = pk4(acc[1][p][f]); vp[p][f] = pk4(acc[2][p][f]); }
        if (w < 3) {
#pragma unroll
            for (int p = 0; p < 2; ++p)
#pragma unroll
                for (int f = 0; f < 5; ++f)
                    *(LAS u32x4*)(xch + (u32)w * 10240u + (u32)((p * 5 + f) * 1024) + (u32)lane * 16u) = cat(kp[p][f], vp[p][f]);
        } else {
#pragma unroll
            for (int p = 0; p < 2; ++p)
#pragma unroll
                for (int f = 0; f < 5; ++f)
                    __builtin_amdgcn_raw_buffer_store_b128(cat(kp[p][f], vp[p][f]), rsWS, lane * 16 + (p * 5 + f) * 1024, WS_P1 + wg * 10240, 16);
        }
    } else {
        u32x4 PK[5], VV[5];
#pragma unroll
        for (int f = 0; f < 5; ++f) {
            u32x2 a[2];
#pragma unroll
            for (int p = 0; p < 2; ++p) {
                f32x4 k4 = acc[1][p][f];
                f32x4 r4 = {phi(rbf(k4[0])), phi(rbf(k4[1])), phi(rbf(k4[2])), phi(rbf(k4[3]))};
                a[p] = pk4(r4);
            }
            PK[f] = cat(a[0], a[1]);
            VV[f] = cat(pk4(acc[2][0][f]), pk4(acc[2][1][f]));
        }
        const u32x4 ones = {0x3F803F80u, 0x3F803F80u, 0x3F803F80u, 0x3F803F80u};
        const f32x4 z4 = {0.f, 0.f, 0.f, 0.f};
        f32x4 G[8];
        const int hc = c16 >> 2;
#pragma unroll
        for (int h = 0; h < 4; ++h) {
            G[h] = mma(PK[h], VV[h], z4);
            f32x4 d1 = mma(PK[4], VV[h], z4);
            f32x4 d2 = mma(PK[h], VV[4], z4);
            f32x4 zz = mma(PK[h], ones, z4);
            if (h == 0) { G[4] = d1; G[5] = d2; G[6] = zz; }
            else {
#pragma unroll
                for (int j = 0; j < 4; ++j) {
                    G[4][j] = gam == h ? d1[j] : G[4][j];
                    G[5][j] = hc == h ? d2[j] : G[5][j];
                    G[6][j] = hc == h ? zz[j] : G[6][j];
                }
            }
        }
        {
            f32x4 d3 = mma(PK[4], VV[4], z4);
            f32x4 zz = mma(PK[4], ones, z4);
#pragma unroll
            for (int j = 0; j < 4; ++j) G[7][j] = gam == hc ? d3[j] : zz[j];
        }
#pragma unroll
        for (int i = 0; i < 8; ++i) *(LAS f32x4*)(xch + (u32)w * 8192u + (u32)(i * 1024) + (u32)lane * 16u) = G[i];
        asm volatile("s_waitcnt lgkmcnt(0)\n\ts_barrier" ::: "memory");
#pragma unroll
        for (int i = 0; i < 2; ++i) {
            f32x4 s = *(LAS f32x4*)(xch + (u32)w * 2048u + (u32)(i * 1024) + (u32)lane * 16u);
#pragma unroll
            for (int ww = 1; ww < 4; ++ww) s += *(LAS f32x4*)(xch + (u32)w * 2048u + (u32)(ww * 8192 + i * 1024) + (u32)lane * 16u);
            __builtin_amdgcn_raw_buffer_store_b128(__builtin_bit_cast(u32x4, s), rsWS, lane * 16 + i * 1024, WS_P1 + wg * 10240 + w * 2048, 16);
        }
    }
    TS(3);

    sfor<40, 43>(step);
    wait_vm<0>();
    if (!ISA || w == 3) set_flag(rsWS, myF1, tag, lane);
    sfor<43, 52>(step);
    TS(4);
    u32x4 gl[2][16];
    u32x4 hv[10];
    if constexpr (ISA) {
        if (w == 0 && t > 0) {
            poll(rsWS, WS_F1 + (u32)((wg - 8) * 4 + 3) * 128u, tagp);
            u32 vl = (u32)lane * 16u;
            u32 so = WS_P1 + (u32)(wg - 8) * 10240u;
            sfor<0, 4>([&](auto I) { ld128_sc1<I.v * 1024>(hv[I.v], vl, rsWS, so); });
            sfor<0, 4>([&](auto I) { ld128_sc1<I.v * 1024>(hv[4 + I.v], vl, rsWS, so + 4096); });
            sfor<0, 2>([&](auto I) { ld128_sc1<I.v * 1024>(hv[8 + I.v], vl, rsWS, so + 8192); });
        } else {
#pragma unroll
            for (int i = 0; i < 10; ++i) hv[i] = (u32x4){0u, 0u, 0u, 0u};
        }
    } else {
        poll(rsWS, WS_F1 + (u32)(((16 + c16) * 8 + g) * 4 + w) * 128u, tag);
        u32 vl = (u32)lane * 16u;
        sfor<0, 16>([&](auto TT) {
            u32 so = WS_P1 + (u32)((16 + TT.v) * 8 + g) * 10240u + (u32)w * 2048u;
            ld128_sc1<0>(gl[0][TT.v], vl, rsWS, so);
            ld128_sc1<1024>(gl[1][TT.v], vl, rsWS, so);
        });
    }
    TS(5);
    sfor<52, 60>(step);
    TS(6);

    u32x2 op[2][5];
    if constexpr (ISA) {
        u32x2 kq[2][5], vq[2][5];
        if (w > 0) {
#pragma unroll
            for (int i = 0; i < 10; ++i) hv[i] = *(LAS u32x4*)(xch + (u32)(w - 1) * 10240u + (u32)(i * 1024) + (u32)lane * 16u);
        } else {
            wait_vm<0>();
#pragma unroll
            for (int i = 0; i < 10; ++i) pin(hv[i]);
        }
#pragma unroll
        for (int i = 0; i < 10; ++i) { kq[i / 5][i % 5] = (u32x2){hv[i][0], hv[i][1]}; vq[i / 5][i % 5] = (u32x2){hv[i][2], hv[i][3]}; }
        const bool hasprev = (w > 0) || (t > 0);
        f32x4 cown, cprv; u32x2 mown, mprv;
        {
            bool vo_[4], vp_[4];
#pragma unroll
            for (int j = 0; j < 4; ++j) {
                int kk = 4 * gam + j;
                vo_[j] = (kk <= c16) && (kk >= c16 - 7);
                vp_[j] = (kk >= c16 + 9) && hasprev;
                cown[j] = vo_[j] ? 0.f : -1e30f;
                cprv[j] = vp_[j] ? 0.f : -1e30f;
            }
            mown[0] = (vo_[0] ? 0xFFFFu : 0u) | (vo_[1] ? 0xFFFF0000u : 0u);
            mown[1] = (vo_[2] ? 0xFFFFu : 0u) | (vo_[3] ? 0xFFFF0000u : 0u);
            mprv[0] = (vp_[0] ? 0xFFFFu : 0u) | (vp_[1] ? 0xFFFF0000u : 0u);
            mprv[1] = (vp_[2] ? 0xFFFFu : 0u) | (vp_[3] ? 0xFFFF0000u : 0u);
        }
        const float cs = 0.22360679774997896f * 1.4426950408889634f;
        const u32x4 ones = {0x3F803F80u, 0x3F803F80u, 0x3F803F80u, 0x3F803F80u};
        const f32x4 z4 = {0.f, 0.f, 0.f, 0.f};
#pragma unroll
        for (int p = 0; p < 2; ++p) {
            u32x2 qp[5];
#pragma unroll
            for (int f = 0; f < 5; ++f) qp[f] = pk4(acc[0][p][f]);
            f32x4 o4s; float r4s;
#pragma unroll
            for (int h = 0; h < 4; ++h) {
                u32x2 q4 = {gam == h ? qp[4][0] : 0u, gam == h ? qp[4][1] : 0u};
                u32x4 Qf = cat(qp[h], q4);
                f32x4 so = mma(cat(kp[p][h], kp[p][4]), Qf, cown);
                f32x4 sp = mma(cat(kq[p][h], kq[p][4]), Qf, cprv);
                f32x4 L;
#pragma unroll
                for (int j = 0; j < 4; ++j) L[j] = vmax(so[j], sp[j]);
                float m = xmax(vmax(vmax3(L[0], L[1], L[2]), L[3]));
                float mc = m * cs;
                f32x4 pr;
#pragma unroll
                for (int j = 0; j < 4; ++j) pr[j] = __builtin_amdgcn_exp2f(__builtin_fmaf(L[j], cs, -mc));
                u32x2 pp = pk4(pr);
                u32x4 Pf = {pp[0] & mown[0], pp[1] & mown[1], pp[0] & mprv[0], pp[1] & mprv[1]};
                f32x4 oh = mma(cat(vp[p][h], vq[p][h]), Pf, z4);
                f32x4 o4 = mma(cat(vp[p][4], vq[p][4]), Pf, z4);
                f32x4 dn = mma(ones, Pf, z4);
                float r = __builtin_amdgcn_rcpf(dn[0]);
                op[p][h] = pk4(oh * r);
                if (h == 0) { o4s = o4; r4s = r; }
                else {
#pragma unroll
                    for (int j = 0; j < 4; ++j) o4s[j] = gam == h ? o4[j] : o4s[j];
                    r4s = gam == h ? r : r4s;
                }
            }
            op[p][4] = pk4(o4s * r4s);
        }
    } else {
        u32x2 qp[2][5];
#pragma unroll
        for (int p = 0; p < 2; ++p)
#pragma unroll
            for (int f = 0; f < 5; ++f) {
                f32x4 q4 = acc[0][p][f];
                f32x4 r4 = {phi(rbf(q4[0])), phi(rbf(q4[1])), phi(rbf(q4[2])), phi(rbf(q4[3]))};
                qp[p][f] = pk4(r4);
            }
        wait_vm<0>();
#pragma unroll
        for (int i = 0; i < 2; ++i) {
#pragma unroll
            for (int tt = 0; tt < 16; ++tt) pin(gl[i][tt]);
            f32x4 s = __builtin_bit_cast(f32x4, gl[i][0]);
#pragma unroll
            for (int tt = 1; tt < 16; ++tt) s += __builtin_bit_cast(f32x4, gl[i][tt]);
            *(LAS u32x2*)(xch + (u32)(w * 2 + i) * 512u + (u32)lane * 8u) = pk4(s);
        }
        asm volatile("s_waitcnt lgkmcnt(0)\n\ts_barrier" ::: "memory");
        u32x2 G[8], zh[4];
#pragma unroll
        for (int i = 0; i < 8; ++i) G[i] = *(LAS u32x2*)(xch + (u32)(i * 512) + (u32)lane * 8u);
#pragma unroll
        for (int h = 0; h < 4; ++h) zh[h] = *(LAS u32x2*)(xch + (u32)(6 * 512 + h * 32) + (u32)gam * 128u);
        u32x2 z4x = *(LAS u32x2*)(xch + (u32)(7 * 512) + (u32)gam * 128u + (u32)(((gam + 1) & 3) * 32));
        const int hc = c16 >> 2;
        const f32x4 z4 = {0.f, 0.f, 0.f, 0.f};
        const u32x2 zz = {0u, 0u};
        u32x4 A1[4], A2[4], A3[4];
#pragma unroll
        for (int h = 0; h < 4; ++h) {
            A1[h] = cat(G[h], gam == h ? G[4] : zz);
            A2[h] = cat(hc == h ? G[5] : zz, (hc == h && gam == h) ? G[7] : zz);
            A3[h] = cat(zh[h], gam == h ? z4x : zz);
        }
#pragma unroll
        for (int p = 0; p < 2; ++p) {
            f32x4 n4 = z4; float r4s;
#pragma unroll
            for (int h = 0; h < 4; ++h) {
                u32x4 Qf = cat(qp[p][h], qp[p][4]);
                f32x4 nh = mma(A1[h], Qf, z4);
                n4 = mma(A2[h], Qf, n4);
                f32x4 dn = mma(A3[h], Qf, z4);
                float r = __builtin_amdgcn_rcpf(dn[0] + 1e-6f);
                op[p][h] = pk4(nh * r);
                r4s = (h == 0 || gam == h) ? r : r4s;
            }
            op[p][4] = pk4(n4 * r4s);
        }
    }
    TS(7);

    const u32 obase = WS_O + (u32)(((b * 16 + t) * 4 + w) * 2) * 8u * 2560u;
#pragma unroll
    for (int p = 0; p < 2; ++p) {
        const u32 so = obase + (u32)(p * 8 + g) * 2560u;
        __builtin_amdgcn_raw_buffer_store_b128(cat(op[p][0], op[p][1]), rsWS, lane * 16, so, 16);
        __builtin_amdgcn_raw_buffer_store_b128(cat(op[p][2], op[p][3]), rsWS, lane * 16 + 1024, so, 16);
        __builtin_amdgcn_raw_buffer_store_b64(op[p][4], rsWS, lane * 8 + 2048, so, 16);
    }
    wait_vm<0>();
    set_flag(rsWS, myF2, tag, lane);
    TS(8);
    poll(rsWS, WS_F2 + (u32)(((b * 16 + t) * 8 + (lane & 7)) * 4 + w) * 128u, tag);
    TS(9);
    {
        u32 vl = (u32)lane * 16u, vl8 = (u32)lane * 8u;
        u32x2 hh[2][8];
        sfor<0, 8>([&](auto GG) {
            sfor<0, 2>([&](auto P) {
                u32 so = obase + (u32)(P.v * 8 + GG.v) * 2560u;
                ld128_sc1<0>(xf[P.v][2 * GG.v], vl, rsWS, so);
                ld128_sc1<1024>(xf[P.v][2 * GG.v + 1], vl, rsWS, so);
            });
        });
        sfor<0, 8>([&](auto GG) {
            sfor<0, 2>([&](auto P) {
                u32 so = obase + (u32)(P.v * 8 + GG.v) * 2560u;
                ld64_sc1<2048>(hh[P.v][GG.v], vl8, rsWS, so);
            });
        });
        if (g == 0 && w == 0) {
            if constexpr (ISA) {
                if (t > 0) set_flag(rsWS, WS_EPOCH + (u32)(128 * t), tagp, lane);
                if (t == 15) set_flag(rsWS, WS_EPOCH + (u32)(128 * 16), tag, lane);
            } else {
                if (t == 0) set_flag(rsWS, WS_EPOCH, tag, lane);
            }
        }
#pragma unroll
        for (int p = 0; p < 2; ++p)
#pragma unroll
            for (int f = 0; f < 5; ++f) acc[0][p][f] = (f32x4){0.f, 0.f, 0.f, 0.f};
        sfor<0, 4>([&](auto SG) {
            wait_vm<48 - 8 * (SG.v + 1)>();
            pinX(IC<SG.v>{});
            sfor<60 + 4 * SG.v, 64 + 4 * SG.v>(step);
        });
        wait_vm<0>();
#pragma unroll
        for (int p = 0; p < 2; ++p)
#pragma unroll
            for (int i = 0; i < 4; ++i) { pin(hh[p][2 * i]); pin(hh[p][2 * i + 1]); xf[p][16 + i] = cat(hh[p][2 * i], hh[p][2 * i + 1]); }
        sfor<76, 80>(step);
    }
    TS(10);
    {
        const rsrc_t rsO = mk_rsrc(outg + (size_t)(t * 128 + w * 32) * (2 * DM) + b * DM + g * 80, 32 * 2 * DM * 2);
        const u32 vrow = (u32)(c16 * 5120);
#pragma unroll
        for (int p = 0; p < 2; ++p) {
#pragma unroll
            for (int f = 0; f < 4; ++f)
                __builtin_amdgcn_raw_buffer_store_b64(pk4(acc[0][p][f]), rsO, vrow + gam * 8 + (p * 2560 + f * 40), 0, 0);
            __builtin_amdgcn_raw_buffer_store_b64(pk4(acc[0][p][4]), rsO, vrow + gam * 40 + (p * 2560 + 32), 0, 0);
        }
    }
    TS(11);
}

__global__ __launch_bounds__(256, 1) void fused_kernel(const u16* __restrict__ xa, const u16* __restrict__ wa, const u16* __restrict__ woa,
                                                       const u16* __restrict__ xb, const u16* __restrict__ wb, const u16* __restrict__ wob,
                                                       u16* __restrict__ out, char* ws) {
    __shared__ __attribute__((aligned(16))) char smem[LDS_B];
    const int x = blockIdx.x & 7, idx = blockIdx.x >> 3;
    const int t = (x & 3) * 4 + (idx >> 3), g = idx & 7;
    if (x < 4) body<true>(xa, wa, woa, out, ws, t, g, smem);
    else       body<false>(xb, wb, wob, out, ws, t, g, smem);
}

extern "C" void kernel_launch(void* const* d_in, const int* in_sizes, int n_in,
                              void* d_out, int out_size, void* d_ws, size_t ws_size,
                              hipStream_t stream) {
    fused_kernel<<<256, 256, 0, stream>>>((const u16*)d_in[0], (const u16*)d_in[1], (const u16*)d_in[2],
                                          (const u16*)d_in[3], (const u16*)d_in[4], (const u16*)d_in[5],
                                          (u16*)d_out, (char*)d_ws);
}
```

```cpp
#include <hip/hip_runtime.h>
#include <stdint.h>

typedef unsigned short u16;
typedef unsigned int u32;
typedef unsigned long long u64;
typedef __attribute__((ext_vector_type(4))) short s16x4;
typedef __attribute__((ext_vector_type(8))) __bf16 bf16x8;
typedef __attribute__((ext_vector_type(2))) __bf16 bf16x2;
typedef __attribute__((ext_vector_type(4))) float f32x4;
typedef __attribute__((ext_vector_type(2))) float f32x2;
typedef __attribute__((ext_vector_type(4))) u32 u32x4;
typedef __attribute__((ext_vector_type(2))) u32 u32x2;
typedef __amdgpu_buffer_rsrc_t rsrc_t;
#define LAS __attribute__((address_space(3)))
#define GAS __attribute__((address_space(1)))
#define DI __device__ __forceinline__
#define HD __host__ __device__
#define SB() __builtin_amdgcn_sched_barrier(0)

#define XMAP 2

constexpr int DM = 640;
constexpr int SLOT_B = 20480;
constexpr int XST_OFF = 4 * SLOT_B;
constexpr int XCH_OFF = 131072;
constexpr int LDS_B = 163840;

constexpr u32 WS_EPOCH = 0;
constexpr u32 WS_F1 = 4096;
constexpr u32 WS_F2 = WS_F1 + 256 * 4 * 128;
constexpr u32 WS_P1 = WS_F2 + 256 * 4 * 128;
constexpr u32 WS_O = WS_P1 + 256 * 10240;
constexpr u32 WS_TS = WS_O + 2 * 16 * 4 * 2 * 8 * 2560;
constexpr u32 WS_BYTES = WS_TS + 256 * 4 * 16 * 8;

constexpr int NST = 20;
HD constexpr int st_type(int s)  { constexpr int T[NST] = {1,1,2,1,2,1,2,1,2,2,0,0,0,0,0,3,3,3,3,3}; return T[s]; }
HD constexpr int st_chunk(int s) { constexpr int C[NST] = {0,1,0,2,1,3,2,4,3,4,0,1,2,3,4,0,1,2,3,4}; return C[s]; }
HD constexpr int st_slot(int s)  { constexpr int C[NST] = {0,1,2,3,0,1,2,3,0,1,2,4,5,3,0,1,2,4,5,3}; return C[s]; }
HD constexpr int xrd_chunk(int s) { return s == 0 ? 1 : s == 2 ? 2 : s == 4 ? 3 : s == 6 ? 4 : -1; }

constexpr int NMG = 300;
struct Sched {
    int kind[NMG];
    int id[NMG];
    int piece[NMG];
    int n_init, n_x[NST], n_sync[NST];
    int left_stage, left_piece;
};
HD constexpr Sched make_sched() {
    Sched r{};
    int uk[32] = {}, ui[32] = {}, ua[32] = {}, us[32] = {}; int nu = 0;
    auto add = [&](int k, int i, int a, int sp) { uk[nu] = k; ui[nu] = i; ua[nu] = a; us[nu] = sp; ++nu; };
    auto aftersync = [](int s) { return 20 * s + 15; };
    add(1, 2, 0, 1); add(1, 3, 0, 1); add(2, 2, 0, 1);
    add(2, 3, 20 * 1, 1);
    add(1, 4, aftersync(1), 1); add(1, 5, aftersync(2), 1);
    add(2, 4, 20 * 3, 1);
    add(1, 6, aftersync(3), 1); add(1, 7, aftersync(4), 1); add(1, 8, aftersync(5), 2); add(1, 9, aftersync(6), 2);
    add(1, 10, aftersync(7), 2); add(1, 11, aftersync(7), 2); add(1, 12, aftersync(7), 2);
    add(1, 13, aftersync(8), 2); add(1, 14, aftersync(9), 2);
    add(1, 15, aftersync(10), 3); add(1, 16, aftersync(11), 3); add(1, 17, aftersync(12), 3); add(1, 18, aftersync(13), 3);
    add(1, 19, aftersync(14), 1);
    int endW[NST] = {}, endX[5] = {};
    int pos = 0;
    pos += 5; endW[0] = pos; pos += 8; endX[0] = pos;
    pos += 5; endW[1] = pos; pos += 8; endX[1] = pos; pos += 2;
    r.n_init = pos - endX[0];
    int u = 0, pc = 0, nextok = 0;
    for (int m = 0; m < NMG; ++m) {
        int s = m / 20, q = m % 20;
        if (q == 10 && xrd_chunk(s) >= 0) r.n_x[s] = pos - endX[xrd_chunk(s)];
        if (q == 15) r.n_sync[s] = pos - endW[s + 1];
        if (u < nu && m >= ua[u] && m >= nextok) {
            r.kind[m] = uk[u]; r.id[m] = ui[u]; r.piece[m] = pc;
            ++pos; ++pc; nextok = m + us[u];
            int np = uk[u] == 1 ? 5 : 8;
            if (pc == np) { if (uk[u] == 1) endW[ui[u]] = pos; else endX[ui[u]] = pos; pc = 0; ++u; }
        }
    }
    r.left_stage = u < nu ? ui[u] : NST; r.left_piece = pc;
    return r;
}
constexpr Sched SCHED = make_sched();
static_assert(SCHED.left_stage == NST, "all LDS-DMA pieces must be placed");

template <int V> struct IC { static constexpr int v = V; };
template <int I, int N, class F> DI void sfor(F&& f) { if constexpr (I < N) { f(IC<I>{}); sfor<I + 1, N>(f); } }

DI f32x4 mma(u32x4 a, u32x4 b, f32x4 c) {
    return __builtin_amdgcn_mfma_f32_16x16x32_bf16(__builtin_bit_cast(bf16x8, a), __builtin_bit_cast(bf16x8, b), c, 0, 0, 0);
}
template <bool TRANS, bool FIRST> DI void mml(f32x4& d, const u32x4& x, const u32x4& w) {
    if constexpr (FIRST) {
        if constexpr (TRANS) asm volatile("v_mfma_f32_16x16x32_bf16 %0, %2, %1, 0" : "=&v"(d) : "a"(x), "v"(w));
        else                 asm volatile("v_mfma_f32_16x16x32_bf16 %0, %1, %2, 0" : "=&v"(d) : "a"(x), "v"(w));
    } else {
        if constexpr (TRANS) asm volatile("v_mfma_f32_16x16x32_bf16 %0, %2, %1, %0" : "+v"(d) : "a"(x), "v"(w));
        else                 asm volatile("v_mfma_f32_16x16x32_bf16 %0, %1, %2, %0" : "+v"(d) : "a"(x), "v"(w));
    }
}
DI u32 pk2(float a, float b) { f32x2 v = {a, b}; return __builtin_bit_cast(u32, __builtin_convertvector(v, bf16x2)); }
DI u32x2 pk4(f32x4 v) { u32x2 r = {pk2(v[0], v[1]), pk2(v[2], v[3])}; return r; }
DI u32x4 cat(u32x2 a, u32x2 b) { u32x4 r = {a[0], a[1], b[0], b[1]}; return r; }
DI u32x2 trread(u32 addr) { return __builtin_bit_cast(u32x2, __builtin_amdgcn_ds_read_tr16_b64_v4i16((LAS s16x4*)(size_t)addr)); }
template <class T> DI T lds_ld(u32 addr) { return *(LAS T*)(size_t)addr; }
template <class T> DI void lds_st(u32 addr, T v) { *(LAS T*)(size_t)addr = v; }
DI float vmax(float a, float b) { return __builtin_elementwise_maximum(a, b); }
DI float xmax(float m) {
    u32 a = __float_as_uint(m);
    auto r = __builtin_amdgcn_permlane16_swap(a, a, false, false);
    m = vmax(__uint_as_float(r[0]), __uint_as_float(r[1]));
    a = __float_as_uint(m);
    auto r2 = __builtin_amdgcn_permlane32_swap(a, a, false, false);
    return vmax(__uint_as_float(r2[0]), __uint_as_float(r2[1]));
}
DI rsrc_t mk_rsrc(const void* p, u32 bytes) { return __builtin_amdgcn_make_buffer_rsrc((void*)p, (short)0, (int)bytes, 0x00020000); }
DI u64 mix(u64 e) { return e * 0x9E3779B97F4A7C15ull + 0x632BE59BD9B4E019ull; }

template <int OFF> DI void ld128_sc1(u32x4& d, u32 voff, rsrc_t rs, u32 soff) {
    asm volatile("buffer_load_dwordx4 %0, %1, %2, %3 offen offset:%4 sc1" : "=v"(d) : "v"(voff), "s"(rs), "s"(soff), "i"(OFF));
}
template <int OFF> DI void ld128a_sc1(u32x4& d, u32 voff, rsrc_t rs, u32 soff) {
    asm volatile("buffer_load_dwordx4 %0, %1, %2, %3 offen offset:%4 sc1" : "=a"(d) : "v"(voff), "s"(rs), "s"(soff), "i"(OFF));
}
template <int OFF> DI void ld64_sc1(u32x2& d, u32 voff, rsrc_t rs, u32 soff) {
    asm volatile("buffer_load_dwordx2 %0, %1, %2, %3 offen offset:%4 sc1" : "=v"(d) : "v"(voff), "s"(rs), "s"(soff), "i"(OFF));
}
template <int OFF> DI void ld64a_sc1(u32x2& d, u32 voff, rsrc_t rs, u32 soff) {
    asm volatile("buffer_load_dwordx2 %0, %1, %2, %3 offen offset:%4 sc1" : "=a"(d) : "v"(voff), "s"(rs), "s"(soff), "i"(OFF));
}
DI void st128_sc1(u32x4 v, rsrc_t rs, u32 voff) { __builtin_amdgcn_raw_buffer_store_b128(v, rs, voff, 0, 16); }
DI void st64_sc1(u32x2 v, rsrc_t rs, u32 voff) { __builtin_amdgcn_raw_buffer_store_b64(v, rs, voff, 0, 16); }
template <int LDSOFF> DI void set_m0(u32 base) { asm volatile("s_add_u32 m0, %0, %1" :: "s"(base), "i"(LDSOFF) : "scc"); }
DI void glds_go(u32 voff, rsrc_t rs, u32 soff) { asm volatile("buffer_load_dwordx4 %0, %1, %2 offen lds" :: "v"(voff), "s"(rs), "s"(soff)); }
template <int LDSOFF> DI void glds(u32 base, u32 voff, rsrc_t rs, u32 soff) {
    asm volatile("s_add_u32 m0, %0, %4\n\ts_nop 0\n\tbuffer_load_dwordx4 %1, %2, %3 offen lds"
                 :: "s"(base), "v"(voff), "s"(rs), "s"(soff), "i"(LDSOFF) : "scc");
}
template <int N> DI void wait_vm() { asm volatile("s_waitcnt vmcnt(%0)" :: "i"(N) : "memory"); }
DI void pin(u32x4& a) { asm volatile("" : "+v"(a)); }
DI void pin(u32x2& a) { asm volatile("" : "+v"(a)); }
DI void pina(u32x4& a) { asm volatile("" : "+a"(a)); }
DI void pina(u32x2& a) { asm volatile("" : "+a"(a)); }

DI void poll(rsrc_t ws, u32 voff, u64 tag) {
#pragma unroll 1
    for (int it = 0; it < (1 << 18); ++it) {
        u32x2 v = __builtin_amdgcn_raw_buffer_load_b64(ws, voff, 0, 16);
        bool ok = (v[0] == (u32)tag) & (v[1] == (u32)(tag >> 32));
        if (__builtin_amdgcn_ballot_w64(!ok) == 0) break;
        __builtin_amdgcn_s_sleep(1);
    }
    asm volatile("" ::: "memory");
}
DI void set_flag(rsrc_t ws, u32 off, u64 tag, int lane) {
    if (lane == 0) { u32x2 v = {(u32)tag, (u32)(tag >> 32)}; __builtin_amdgcn_raw_buffer_store_b64(v, ws, off, 0, 16); }
}

#define TS(i) do {} while (0)

DI float phi(float t) {
    float e = __builtin_amdgcn_fmed3f(__builtin_amdgcn_exp2f(t * 1.4426950408889634f), 0.f, 1.f);
    return vmax(t + 1.0f, e) + 1e-6f;
}
DI u32x2 phi4(f32x4 v) {
    u32x2 r = pk4(v);
    f32x4 t = {__uint_as_float(r[0] << 16), __uint_as_float(r[0] & 0xFFFF0000u), __uint_as_float(r[1] << 16), __uint_as_float(r[1] & 0xFFFF0000u)};
    f32x4 o = {phi(t[0]), phi(t[1]), phi(t[2]), phi(t[3])};
    return pk4(o);
}

template <bool ISA>
DI void body(const u16* __restrict__ xg, const u16* __restrict__ wg_, const u16* __restrict__ wog, u16* __restrict__ outg,
             char* wsp, int t, int g, char* smem) {
    const int tid = threadIdx.x;
    const int lane = tid & 63;
    const int w = __builtin_amdgcn_readfirstlane(tid >> 6);
    const int gam = lane >> 4, c16 = lane & 15;
    const int b = ISA ? 0 : 1;
    const int wg = (b * 16 + t) * 8 + g;
    const u32 lds0 = (u32)(size_t)(LAS char*)smem;

    const rsrc_t rsW = mk_rsrc(wg_, DM * 3 * DM * 2);
    const rsrc_t rsX = mk_rsrc(xg + (size_t)(t * 128 + w * 32) * DM, 32 * DM * 2);
    const rsrc_t rsWo = mk_rsrc(wog, DM * DM * 2);
    const rsrc_t rsWS = mk_rsrc(wsp, WS_BYTES);
    TS(0);

    u32 vw[5];
#pragma unroll
    for (int i = 0; i < 5; ++i) {
        int c = i * 64 + lane;
        int rl = c / 10, ch = c - rl * 10;
        int k = w * 32 + (rl ^ ((rl >> 1) & 4));
        vw[i] = (u32)(k * 3840 + ch * 16);
    }
    const u32 ldsw = lds0 + (u32)w * 5120u;
    const u32 ldsx = lds0 + XST_OFF + (u32)w * 16384u;
    const int rot = (t + g) % 5;
    u32 rck[5];
#pragma unroll
    for (int j = 0; j < 5; ++j) { int c = j + rot; c = c >= 5 ? c - 5 : c; rck[j] = (u32)c; }
    auto soffW = [&](auto S) { return rck[st_chunk(S.v)] * (128u * 3840u) + (u32)(st_type(S.v) * 1280) + (u32)g * 160u; };
    auto issueW = [&](auto S) { const u32 so = soffW(S); sfor<0, 5>([&](auto I) { glds<st_slot(S.v) * SLOT_B + I.v * 1024>(ldsw, vw[I.v], rsW, so); }); };
    issueW(IC<0>{});
    u32 vx[8];
#pragma unroll
    for (int j = 0; j < 8; ++j) { int row = 4 * j + gam; vx[j] = (u32)(row * 1280 + ((c16 ^ (row >> 1)) * 16)); }
    auto issueX = [&](auto C) { const u32 so = rck[C.v] * 256u; sfor<0, 8>([&](auto J) { glds<(C.v & 1) * 8192 + J.v * 1024>(ldsx, vx[J.v], rsX, so); }); };
    issueX(IC<0>{}); issueW(IC<1>{}); issueX(IC<1>{});
    u32x2 ep0, ep1;
    {
        u32 o0 = ISA ? (u32)(128 * (1 + t)) : 0u;
        u32 o1 = ISA ? (u32)(128 * t) : 0u;
        u32 z = 0;
        ld64_sc1<0>(ep0, z, rsWS, o0);
        ld64_sc1<0>(ep1, z, rsWS, o1);
    }

    u32 vo[5], vo4[5];
#pragma unroll
    for (int i = 0; i < 5; ++i) {
        int c = i * 64 + lane;
        int rl = c / 10, ch = c - rl * 10;
        int k = rl ^ ((rl >> 1) & 4);
        int ga = k >> 3, e = k & 7;
        int row = 20 * (2 * w + (e >> 2)) + 4 * ga + (e & 3);
        int row4 = 20 * (8 * w + 4 * (e >> 2) + ga) + 16 + (e & 3);
        vo[i] = (u32)(row * 1280 + ch * 16);
        vo4[i] = (u32)(row4 * 1280 + ch * 16);
        asm volatile("" : "+v"(vo[i]), "+v"(vo4[i]));
    }

    u32 rb[2][2][2];
    {
        int q = (lane >> 2) & 3, p = lane & 3;
#pragma unroll
        for (int tt = 0; tt < 2; ++tt) {
            int row = 8 * gam + 4 * (tt ^ (gam & 1)) + q;
            rb[0][tt][0] = lds0 + (u32)(row * 160 + 8 * p);
            rb[0][tt][1] = lds0 + (u32)(row * 160 + 40 * p + 32);
            rb[1][tt][0] = rb[0][tt][0] + 3 * SLOT_B;
            rb[1][tt][1] = rb[0][tt][1] + 3 * SLOT_B;
            asm volatile("" : "+v"(rb[0][tt][0]), "+v"(rb[0][tt][1]), "+v"(rb[1][tt][0]), "+v"(rb[1][tt][1]));
        }
    }
    u32x2 Bh[2][5][2];
    auto readB = [&](auto N, auto F, auto T) {
        constexpr int n = N.v, s = n >> 2, kk = n & 3, slot = st_slot(s), hf = slot / 3, f = F.v;
        constexpr int off = (slot % 3) * SLOT_B + kk * 5120;
        Bh[n & 1][f][T.v] = f < 4 ? trread(rb[hf][T.v][0] + off + 40 * f) : trread(rb[hf][T.v][1] + off);
    };
    u32 rx[4];
#pragma unroll
    for (int kk = 0; kk < 4; ++kk) { rx[kk] = ldsx + (u32)(c16 * 512 + (((4 * kk + gam) ^ c16) * 16)); asm volatile("" : "+v"(rx[kk])); }
    u32x4 xf[2][20];
    auto readX = [&](auto C, auto J) {
        constexpr int c = C.v, p = J.v & 1, kk = J.v >> 1;
        xf[p][c * 4 + kk] = lds_ld<u32x4>(rx[kk] + (c & 1) * 8192 + p * 256);
    };

    f32x4 acc[3][2][5];

    SB();
    wait_vm<SCHED.n_init>();
    sfor<0, 8>([&](auto J) { readX(IC<0>{}, J); });
    asm volatile("s_barrier" ::: "memory");
    TS(1);
    sfor<0, 5>([&](auto F) { readB(IC<0>{}, F, IC<0>{}); readB(IC<0>{}, F, IC<1>{}); });
    asm volatile("s_waitcnt lgkmcnt(10)" ::: "memory");
    SB();

    auto step = [&](auto N) {
        constexpr int n = N.v, s = n >> 2, kk = n & 3, ty = st_type(s), ck = st_chunk(s);
        constexpr int ai = ty == 3 ? 0 : ty;
        constexpr bool trans = (ty == 0 || ty == 3) || (ISA && ty == 1);
        constexpr bool first = ck == 0 && kk == 0;
        constexpr int xc = xrd_chunk(s);
        if constexpr (kk == 3 && s < 15) {
            asm volatile("s_waitcnt vmcnt(%0)\n\ts_barrier" :: "i"(SCHED.n_sync[s]) : "memory");
            SB();
        }
        if constexpr (kk == 2 && xc >= 0) { wait_vm<SCHED.n_x[s]>(); SB(); }
        sfor<0, 5>([&](auto F) {
            constexpr int f = F.v, m = n * 5 + f;
            constexpr int vk = m < NMG ? SCHED.kind[m < NMG ? m : 0] : 0;
            constexpr int vi = SCHED.id[m < NMG ? m : 0], vp = SCHED.piece[m < NMG ? m : 0];
            const u32x4 Bw = cat(Bh[n & 1][f][0], Bh[n & 1][f][1]);
            if constexpr (vk == 1) set_m0<st_slot(vi) * SLOT_B + vp * 1024>(ldsw);
            if constexpr (vk == 2) set_m0<(vi & 1) * 8192 + vp * 1024>(ldsx);
            mml<trans, first>(acc[ai][0][f], xf[0][ck * 4 + kk], Bw);
            SB();
            if constexpr (n + 1 < NST * 4) readB(IC<n + 1>{}, F, IC<0>{});
            if constexpr (vk == 1) {
                if constexpr (st_type(vi) == 3) glds_go(st_chunk(vi) < 4 ? vo[vp] : vo4[vp], rsWo, (u32)(st_chunk(vi) < 4 ? st_chunk(vi) * 160 * 1280 : 0) + (u32)g * 160u);
                else glds_go(vw[vp], rsW, soffW(IC<vi>{}));
            }
            if constexpr (vk == 2) glds_go(vx[vp], rsX, rck[vi] * 256u);
            if constexpr (kk == 2 && xc >= 0 && f < 4) readX(IC<xc>{}, IC<2 * f>{});
            SB();
            mml<trans, first>(acc[ai][1][f], xf[1][ck * 4 + kk], Bw);
            SB();
            if constexpr (n + 1 < NST * 4) readB(IC<n + 1>{}, F, IC<1>{});
            if constexpr (kk == 2 && xc >= 0 && f < 4) readX(IC<xc>{}, IC<2 * f + 1>{});
            SB();
        });
    };
    auto settle = [&](f32x4 (&a)[2][5]) {
        asm volatile("s_nop 7" : "+v"(a[0][0]), "+v"(a[0][1]), "+v"(a[0][2]), "+v"(a[0][3]), "+v"(a[0][4]),
                                 "+v"(a[1][0]), "+v"(a[1][1]), "+v"(a[1][2]), "+v"(a[1][3]), "+v"(a[1][4]));
    };

    sfor<0, 40>(step);
    settle(acc[1]); settle(acc[2]);
    TS(2);
    pin(ep0); pin(ep1);
    const u64 tag = mix(((u64)(u32)__builtin_amdgcn_readfirstlane(ep0[1]) << 32) | (u32)__builtin_amdgcn_readfirstlane(ep0[0]));
    const u64 tagp = mix(((u64)(u32)__builtin_amdgcn_readfirstlane(ep1[1]) << 32) | (u32)__builtin_amdgcn_readfirstlane(ep1[0]));
    const u32 myF1 = WS_F1 + (u32)(wg * 4 + w) * 128u;
    const u32 myF2 = WS_F2 + (u32)(wg * 4 + w) * 128u;
    const u32 xch = lds0 + XCH_OFF;
    const u32 vl = (u32)lane * 16u, vl8 = (u32)lane * 8u;

    u32x2 kp[2][5], vp[2][5];
    if constexpr (ISA) {
#pragma unroll
        for (int p = 0; p < 2; ++p)
#pragma unroll
            for (int f = 0; f < 5; ++f) { kp[p][f] = pk4(acc[1][p][f]); vp[p][f] = pk4(acc[2][p][f]); }
        if (w < 3) {
#pragma unroll
            for (int i = 0; i < 10; ++i) lds_st(xch + (u32)w * 10240u + (u32)(i * 1024) + vl, cat(kp[i / 5][i % 5], vp[i / 5][i % 5]));
        } else {
            const u32 vo_ = WS_P1 + (u32)wg * 10240u + vl;
#pragma unroll
            for (int i = 0; i < 10; ++i) st128_sc1(cat(kp[i / 5][i % 5], vp[i / 5][i % 5]), rsWS, vo_ + i * 1024);
        }
    } else {
        u32x4 PK[5], VV[5];
#pragma unroll
        for (int f = 0; f < 5; ++f) {
            PK[f] = cat(phi4(acc[1][0][f]), phi4(acc[1][1][f]));
            VV[f] = cat(pk4(acc[2][0][f]), pk4(acc[2][1][f]));
        }
        const u32x4 ones = {0x3F803F80u, 0x3F803F80u, 0x3F803F80u, 0x3F803F80u};
        const f32x4 z4 = {0.f, 0.f, 0.f, 0.f};
        f32x4 G[8];
        const int hc = c16 >> 2;
#pragma unroll
        for (int h = 0; h < 4; ++h) {
            G[h] = mma(PK[h], VV[h], z4);
            f32x4 d1 = mma(PK[4], VV[h], z4);
            f32x4 d2 = mma(PK[h], VV[4], z4);
            f32x4 zz = mma(PK[h], ones, z4);
            if (h == 0) { G[4] = d1; G[5] = d2; G[6] = zz; }
            else {
#pragma unroll
                for (int j = 0; j < 4; ++j) {
                    G[4][j] = gam == h ? d1[j] : G[4][j];
                    G[5][j] = hc == h ? d2[j] : G[5][j];
                    G[6][j] = hc == h ? zz[j] : G[6][j];
                }
            }
        }
        {
            f32x4 d3 = mma(PK[4], VV[4], z4);
            f32x4 zz = mma(PK[4], ones, z4);
#pragma unroll
            for (int j = 0; j < 4; ++j) G[7][j] = gam == hc ? d3[j] : zz[j];
        }
#pragma unroll
        for (int i = 0; i < 8; ++i) lds_st(xch + (u32)w * 8192u + (u32)(i * 1024) + vl, G[i]);
        asm volatile("s_waitcnt lgkmcnt(0)\n\ts_barrier" ::: "memory");
        const u32 vo_ = WS_P1 + (u32)wg * 10240u + (u32)w * 2048u + vl;
#pragma unroll
        for (int i = 0; i < 2; ++i) {
            f32x4 s = lds_ld<f32x4>(xch + (u32)w * 2048u + (u32)(i * 1024) + vl);
#pragma unroll
            for (int ww = 1; ww < 4; ++ww) s += lds_ld<f32x4>(xch + (u32)w * 2048u + (u32)(ww * 8192 + i * 1024) + vl);
            st128_sc1(__builtin_bit_cast(u32x4, s), rsWS, vo_ + i * 1024);
        }
    }
    SB();
    TS(3);

    sfor<40, 43>(step);
    wait_vm<0>();
    if (!ISA || w == 3) set_flag(rsWS, myF1, tag, lane);
    asm volatile("s_waitcnt lgkmcnt(0)" ::: "memory");
    SB();
    sfor<43, 52>(step);
    TS(4);
    u32x4 gl[2][16];
    u32x4 hv[10];
    if constexpr (ISA) {
        if (w == 0 && t > 0) {
            poll(rsWS, WS_F1 + (u32)((wg - 8) * 4 + 3) * 128u, tagp);
            u32 so = WS_P1 + (u32)(wg - 8) * 10240u;
            sfor<0, 4>([&](auto I) { ld128_sc1<I.v * 1024>(hv[I.v], vl, rsWS, so); });
            sfor<0, 4>([&](auto I) { ld128_sc1<I.v * 1024>(hv[4 + I.v], vl, rsWS, so + 4096); });
            sfor<0, 2>([&](auto I) { ld128_sc1<I.v * 1024>(hv[8 + I.v], vl, rsWS, so + 8192); });
        } else {
#pragma unroll
            for (int i = 0; i < 10; ++i) hv[i] = (u32x4){0u, 0u, 0u, 0u};
        }
    } else {
        poll(rsWS, WS_F1 + (u32)(((16 + c16) * 8 + g) * 4 + w) * 128u, tag);
        sfor<0, 16>([&](auto TT) {
            u32 so = WS_P1 + (u32)((16 + TT.v) * 8 + g) * 10240u + (u32)w * 2048u;
            ld128_sc1<0>(gl[0][TT.v], vl, rsWS, so);
            ld128_sc1<1024>(gl[1][TT.v], vl, rsWS, so);
        });
    }
    SB();
    TS(5);
    sfor<52, 60>(step);
    settle(acc[0]);
    TS(6);

    u32x2 op[2][5];
    if constexpr (ISA) {
        u32x2 kq[2][5], vq[2][5];
        if (w > 0) {
#pragma unroll
            for (int i = 0; i < 10; ++i) hv[i] = lds_ld<u32x4>(xch + (u32)(w - 1) * 10240u + (u32)(i * 1024) + vl);
        } else {
            wait_vm<0>();
#pragma unroll
            for (int i = 0; i < 10; ++i) pin(hv[i]);
        }
#pragma unroll
        for (int i = 0; i < 10; ++i) { kq[i / 5][i % 5] = (u32x2){hv[i][0], hv[i][1]}; vq[i / 5][i % 5] = (u32x2){hv[i][2], hv[i][3]}; }
        const bool hasprev = (w > 0) || (t > 0);
        f32x4 cown, cprv; u32x2 mown, mprv;
        {
            bool vo_[4], vp_[4];
#pragma unroll
            for (int j = 0; j < 4; ++j) {
                int kk = 4 * gam + j;
                vo_[j] = (kk <= c16) && (kk >= c16 - 7);
                vp_[j] = (kk >= c16 + 9) && hasprev;
                cown[j] = vo_[j] ? 0.f : -1e30f;
                cprv[j] = vp_[j] ? 0.f : -1e30f;
            }
            mown[0] = (vo_[0] ? 0xFFFFu : 0u) | (vo_[1] ? 0xFFFF0000u : 0u);
            mown[1] = (vo_[2] ? 0xFFFFu : 0u) | (vo_[3] ? 0xFFFF0000u : 0u);
            mprv[0] = (vp_[0] ? 0xFFFFu : 0u) | (vp_[1] ? 0xFFFF0000u : 0u);
            mprv[1] = (vp_[2] ? 0xFFFFu : 0u) | (vp_[3] ? 0xFFFF0000u : 0u);
        }
        const float cs = 0.22360679774997896f * 1.4426950408889634f;
        const u32x4 ones = {0x3F803F80u, 0x3F803F80u, 0x3F803F80u, 0x3F803F80u};
        const f32x4 z4 = {0.f, 0.f, 0.f, 0.f};
#pragma unroll
        for (int p = 0; p < 2; ++p) {
            u32x2 qp[5];
#pragma unroll
            for (int f = 0; f < 5; ++f) qp[f] = pk4(acc[0][p][f]);
            f32x4 o4s; float r4s;
#pragma unroll
            for (int h = 0; h < 4; ++h) {
                u32x2 q4 = {gam == h ? qp[4][0] : 0u, gam == h ? qp[4][1] : 0u};
                u32x4 Qf = cat(qp[h], q4);
                f32x4 so = mma(cat(kp[p][h], kp[p][4]), Qf, cown);
                f32x4 sp = mma(cat(kq[p][h], kq[p][4]), Qf, cprv);
                f32x4 L;
#pragma unroll
                for (int j = 0; j < 4; ++j) L[j] = vmax(so[j], sp[j]);
                float m = xmax(vmax(vmax(vmax(L[0], L[1]), L[2]), L[3]));
                float mc = m * cs;
                f32x4 pr;
#pragma unroll
                for (int j = 0; j < 4; ++j) pr[j] = __builtin_amdgcn_exp2f(__builtin_fmaf(L[j], cs, -mc));
                u32x2 pp = pk4(pr);
                u32x4 Pf = {pp[0] & mown[0], pp[1] & mown[1], pp[0] & mprv[0], pp[1] & mprv[1]};
                f32x4 oh = mma(cat(vp[p][h], vq[p][h]), Pf, z4);
                f32x4 o4 = mma(cat(vp[p][4], vq[p][4]), Pf, z4);
                f32x4 dn = mma(ones, Pf, z4);
                float r = __builtin_amdgcn_rcpf(dn[0]);
                op[p][h] = pk4(oh * r);
                if (h == 0) { o4s = o4; r4s = r; }
                else {
#pragma unroll
                    for (int j = 0; j < 4; ++j) o4s[j] = gam == h ? o4[j] : o4s[j];
                    r4s = gam == h ? r : r4s;
                }
            }
            op[p][4] = pk4(o4s * r4s);
        }
    } else {
        u32x2 qp[2][5];
#pragma unroll
        for (int p = 0; p < 2; ++p)
#pragma unroll
            for (int f = 0; f < 5; ++f) qp[p][f] = phi4(acc[0][p][f]);
        SB();
        wait_vm<0>();
#pragma unroll
        for (int i = 0; i < 2; ++i) {
#pragma unroll
            for (int tt = 0; tt < 16; ++tt) pin(gl[i][tt]);
            f32x4 s = __builtin_bit_cast(f32x4, gl[i][0]);
#pragma unroll
            for (int tt = 1; tt < 16; ++tt) s += __builtin_bit_cast(f32x4, gl[i][tt]);
            lds_st(xch + (u32)(w * 2 + i) * 512u + vl8, pk4(s));
        }
        asm volatile("s_waitcnt lgkmcnt(0)\n\ts_barrier" ::: "memory");
        u32x2 G[8], zh[4];
#pragma unroll
        for (int i = 0; i < 8; ++i) G[i] = lds_ld<u32x2>(xch + (u32)(i * 512) + vl8);
#pragma unroll
        for (int h = 0; h < 4; ++h) zh[h] = lds_ld<u32x2>(xch + (u32)(6 * 512 + h * 32) + (u32)gam * 128u);
        u32x2 z4x = lds_ld<u32x2>(xch + (u32)(7 * 512) + (u32)gam * 128u + (u32)(((gam + 1) & 3) * 32));
        const int hc = c16 >> 2;
        const f32x4 z4 = {0.f, 0.f, 0.f, 0.f};
        const u32x2 zz = {0u, 0u};
        u32x4 A1[4], A2[4], A3[4];
#pragma unroll
        for (int h = 0; h < 4; ++h) {
            A1[h] = cat(G[h], gam == h ? G[4] : zz);
            A2[h] = cat(hc == h ? G[5] : zz, (hc == h && gam == h) ? G[7] : zz);
            A3[h] = cat(zh[h], gam == h ? z4x : zz);
        }
#pragma unroll
        for (int p = 0; p < 2; ++p) {
            f32x4 n4 = z4; float r4s;
#pragma unroll
            for (int h = 0; h < 4; ++h) {
                u32x4 Qf = cat(qp[p][h], qp[p][4]);
                f32x4 nh = mma(A1[h], Qf, z4);
                n4 = mma(A2[h], Qf, n4);
                f32x4 dn = mma(A3[h], Qf, z4);
                float r = __builtin_amdgcn_rcpf(dn[0] + 1e-6f);
                op[p][h] = pk4(nh * r);
                r4s = (h == 0 || gam == h) ? r : r4s;
            }
            op[p][4] = pk4(n4 * r4s);
        }
    }
    SB();
    TS(7);

    const u32 obase = WS_O + (u32)(((b * 16 + t) * 4 + w) * 2) * 8u * 2560u;
#pragma unroll
    for (int p = 0; p < 2; ++p) {
        const u32 so = obase + (u32)(p * 8 + g) * 2560u;
        st128_sc1(cat(op[p][0], op[p][1]), rsWS, so + vl);
        st128_sc1(cat(op[p][2], op[p][3]), rsWS, so + vl + 1024);
        st64_sc1(op[p][4], rsWS, so + vl8 + 2048);
    }
    wait_vm<0>();
    set_flag(rsWS, myF2, tag, lane);
    asm volatile("s_barrier" ::: "memory");
    TS(8);
    poll(rsWS, WS_F2 + (u32)(((b * 16 + t) * 8 + (lane & 7)) * 4 + w) * 128u, tag);
    TS(9);
    {
        u32x2 hh[2][8];
        sfor<0, 8>([&](auto GG) {
            sfor<0, 2>([&](auto P) {
                u32 so = obase + (u32)(P.v * 8 + GG.v) * 2560u;
                ld128a_sc1<0>(xf[P.v][2 * GG.v], vl, rsWS, so);
                ld128a_sc1<1024>(xf[P.v][2 * GG.v + 1], vl, rsWS, so);
            });
        });
        sfor<0, 8>([&](auto GG) {
            sfor<0, 2>([&](auto P) {
                u32 so = obase + (u32)(P.v * 8 + GG.v) * 2560u;
                ld64a_sc1<2048>(hh[P.v][GG.v], vl8, rsWS, so);
            });
        });
        SB();
        sfor<0, 8>([&](auto SG) {
            wait_vm<48 - 4 * (SG.v + 1)>();
            pina(xf[0][2 * SG.v]); pina(xf[0][2 * SG.v + 1]); pina(xf[1][2 * SG.v]); pina(xf[1][2 * SG.v + 1]);
            SB();
            sfor<60 + 2 * SG.v, 62 + 2 * SG.v>(step);
        });
        wait_vm<0>();
#pragma unroll
        for (int p = 0; p < 2; ++p)
#pragma unroll
            for (int i = 0; i < 4; ++i) { pina(hh[p][2 * i]); pina(hh[p][2 * i + 1]); xf[p][16 + i] = cat(hh[p][2 * i], hh[p][2 * i + 1]); }
        asm volatile("s_nop 1" : "+a"(xf[0][16]), "+a"(xf[0][17]), "+a"(xf[0][18]), "+a"(xf[0][19]), "+a"(xf[1][16]), "+a"(xf[1][17]), "+a"(xf[1][18]), "+a"(xf[1][19]));
        SB();
        sfor<76, 80>(step);
        settle(acc[0]);
    }
    TS(10);
    {
        const rsrc_t rsO = mk_rsrc(outg + (size_t)(t * 128 + w * 32) * (2 * DM) + b * DM + g * 80, 32 * 2 * DM * 2);
        const u32 vrow = (u32)(c16 * 5120);
#pragma unroll
        for (int p = 0; p < 2; ++p) {
#pragma unroll
            for (int f = 0; f < 4; ++f)
                __builtin_amdgcn_raw_buffer_store_b64(pk4(acc[0][p][f]), rsO, vrow + gam * 8 + (p * 2560 + f * 40), 0, 0);
            __builtin_amdgcn_raw_buffer_store_b64(pk4(acc[0][p][4]), rsO, vrow + gam * 40 + (p * 2560 + 32), 0, 0);
        }
    }
    if (g == 0 && w == 0) {
        if constexpr (ISA) {
            if (t > 0) set_flag(rsWS, WS_EPOCH + (u32)(128 * t), tagp, lane);
            if (t == 15) set_flag(rsWS, WS_EPOCH + (u32)(128 * 16), tag, lane);
        } else {
            if (t == 0) set_flag(rsWS, WS_EPOCH, tag, lane);
        }
    }
    TS(11);
}

__global__ __launch_bounds__(256, 1) void fused_kernel(const u16* __restrict__ xa, const u16* __restrict__ wa, const u16* __restrict__ woa,
                                                       const u16* __restrict__ xb, const u16* __restrict__ wb, const u16* __restrict__ wob,
                                                       u16* __restrict__ out, char* ws) {
    __shared__ __attribute__((aligned(16))) char smem[LDS_B];
    const int x = blockIdx.x & 7, idx = blockIdx.x >> 3;
    const int t = (x & 3) * 4 + (idx >> 3), g = idx & 7;
    if (x < 4) body<true>(xa, wa, woa, out, ws, t, g, smem);
    else       body<false>(xb, wb, wob, out, ws, t, g, smem);
}

extern "C" void kernel_launch(void* const* d_in, const int* in_sizes, int n_in,
                              void* d_out, int out_size, void* d_ws, size_t ws_size,
                              hipStream_t stream) {
    fused_kernel<<<256, 256, 0, stream>>>((const u16*)d_in[0], (const u16*)d_in[1], (const u16*)d_in[2],
                                          (const u16*)d_in[3], (const u16*)d_in[4], (const u16*)d_in[5],
                                          (u16*)d_out, (char*)d_ws);
}
```

```cpp
#include <hip/hip_runtime.h>
#include <stdint.h>

typedef unsigned short u16;
typedef unsigned int u32;
typedef unsigned long long u64;
typedef __attribute__((ext_vector_type(4))) short s16x4;
typedef __attribute__((ext_vector_type(2))) __bf16 bf16x2;
typedef __attribute__((ext_vector_type(4))) float f32x4;
typedef __attribute__((ext_vector_type(2))) float f32x2;
typedef __attribute__((ext_vector_type(4))) u32 u32x4;
typedef __attribute__((ext_vector_type(2))) u32 u32x2;
typedef __amdgpu_buffer_rsrc_t rsrc_t;
#define LAS __attribute__((address_space(3)))
#define GAS __attribute__((address_space(1)))
#define DI __device__ __forceinline__
#define HD __host__ __device__
#define SB() __builtin_amdgcn_sched_barrier(0)

#define OAUX 0
#define XMAP 1

constexpr int DM = 640;
constexpr int SLOT_B = 20480;
constexpr int XST_OFF = 4 * SLOT_B;
constexpr int XCH_OFF = 6 * SLOT_B;
constexpr int LDS_B = 163840;
constexpr u32 OOB = 0x7FFFFF00u;

constexpr u32 WS_EPOCH = 0;
constexpr u32 WS_F1 = 4096;
constexpr u32 WS_F2 = WS_F1 + 256 * 4 * 128;
constexpr u32 WS_P1 = WS_F2 + 256 * 4 * 128;
constexpr u32 WS_O = WS_P1 + 256 * 8192;
constexpr u32 OBLK = 2 * 20480 + 128;
constexpr u32 WS_TS = WS_O + 2 * 16 * 4 * OBLK;
constexpr u32 WS_TS2 = WS_TS + 256 * 4 * 16 * 8;
constexpr u32 WS_BYTES = WS_TS2 + 256 * 4 * 64 * 4;

constexpr int NST = 20;
HD constexpr int st_type(int s)  { return s < 5 ? 1 : s < 10 ? 2 : s < 15 ? 0 : 3; }
HD constexpr int st_chunk(int s) { return s % 5; }
HD constexpr int st_slot(int s)  { return s < 8 ? s % 4 : (s - 4) % 6; }
HD constexpr int sync_m(int s) { return (s == 4 || s == 9) ? 20 * s + 20 : 20 * s + 15; }
HD constexpr int xrd_half(int n) { return (n & 1) && n < 19 ? (n + 1) / 2 : -1; }

constexpr int NMG = 300;
#ifndef M_WPF
#define M_WPF 130
#endif
constexpr int M_PUBA = 200;
#ifndef M_FLAG
#define M_FLAG 228
#define M_POLL 250
#define M_CHK 272
#endif
constexpr int M_PARK = 293;
#define LOOKA 50
#define OSPACE 5
#define QEARLY 15
#define NOIN 2
struct Sched {
    int kind[NMG], id[NMG], piece[NMG];
    int pos[NMG + 1];
    int n_init, n_x[10], n_sync[NST];
    int left_stage;
};
HD constexpr int xpre(bool isa, int m) { return (!isa && m == 200) ? 1 : m == M_FLAG ? 1 : (isa && m == M_CHK) ? 2 : 0; }
HD constexpr int xop(bool isa, int m) {
    if (m == M_WPF) return 1;
    if (!isa && m >= M_CHK && m < M_CHK + 16) return 1;
    if (isa && m >= M_PUBA + 4 && m < M_PUBA + 6) return 1;
    if (m == M_POLL) return 1;
    return 0;
}
HD constexpr Sched make_sched(bool isa) {
    Sched r{};
    int uk[32] = {}, ui[32] = {}, ua[32] = {}, us[32] = {}; int nu = 0;
    auto add = [&](int k, int i, int a, int sp) { uk[nu] = k; ui[nu] = i; ua[nu] = a; us[nu] = sp; ++nu; };
    auto mx = [](int x, int y) { return x > y ? x : y; };
    auto xav = [&](int h) { return mx(h < 5 ? 0 : h == 5 ? 5 : 5 * (2 * (h - 5) + 1), 5 * (2 * h - 1) - LOOKA); };
    auto wfree = [](int q) { return q < 4 ? 0 : q < 8 ? sync_m(q - 3) : q < 10 ? sync_m(4) : sync_m(q - 5); };
    auto wav = [&](int q) { return mx(wfree(q), sync_m(q - 1) - LOOKA); };
    add(2, 2, xav(2), 1); add(2, 3, xav(3), 1);
    for (int q = 2; q < 5; ++q) { add(1, q, wav(q), 1); add(2, 2 * q, xav(2 * q), 1); add(2, 2 * q + 1, xav(2 * q + 1), 1); }
    for (int q = 5; q < 15; ++q) add(1, q, q < QEARLY ? wav(q) : wfree(q), q < QEARLY ? 1 : 2);
    for (int q = 15; q < 15 + NOIN; ++q) add(1, q, wfree(q), OSPACE);
    int endW[NST] = {}, endX[10] = {};
    int pos = 0;
    pos += 4; endX[0] = pos; pos += 5; endW[0] = pos; pos += 4; endX[1] = pos;
    pos += 5; endW[1] = pos; pos += 5;
    r.n_init = pos - endW[0];
    int u = 0, pc = 0, nextok = 0;
    for (int m = 0; m < NMG; ++m) {
        pos += xpre(isa, m);
        r.pos[m] = pos;
        if (m % 5 == 0 && xrd_half(m / 5) >= 0) r.n_x[xrd_half(m / 5)] = endX[xrd_half(m / 5)] ? pos - endX[xrd_half(m / 5)] : -1000;
        for (int s = 0; s < 15; ++s) if (sync_m(s) == m) r.n_sync[s] = s == 14 ? 63 : endW[s + 1] ? pos - endW[s + 1] : -1000;
        if (u < nu && m >= ua[u] && m >= nextok) {
            r.kind[m] = uk[u]; r.id[m] = ui[u]; r.piece[m] = pc;
            ++pos; ++pc; nextok = m + us[u];
            int np = uk[u] == 1 ? 5 : 4;
            if (pc == np) { if (uk[u] == 1) endW[ui[u]] = pos; else endX[ui[u]] = pos; pc = 0; ++u; }
        }
        pos += xop(isa, m);
    }
    r.pos[NMG] = pos;
    r.left_stage = u < nu ? ui[u] : NST;
    return r;
}
constexpr Sched SCHED_A = make_sched(true), SCHED_B = make_sched(false);
static_assert(SCHED_A.left_stage == NST && SCHED_B.left_stage == NST, "all LDS-DMA pieces must be placed");
HD constexpr int cap63(int n) { return n > 63 ? 63 : n; }

template <int V> struct IC { static constexpr int v = V; };
template <int I, int N, class F> DI void sfor(F&& f) { if constexpr (I < N) { f(IC<I>{}); sfor<I + 1, N>(f); } }

template <bool TRANS, bool FIRST> DI void mml(f32x4& d, const u32x4& x, const u32x4& w) {
    if constexpr (FIRST) {
        if constexpr (TRANS) asm volatile("v_mfma_f32_16x16x32_bf16 %0, %2, %1, 0" : "=&v"(d) : "a"(x), "v"(w));
        else                 asm volatile("v_mfma_f32_16x16x32_bf16 %0, %1, %2, 0" : "=&v"(d) : "a"(x), "v"(w));
    } else {
        if constexpr (TRANS) asm volatile("v_mfma_f32_16x16x32_bf16 %0, %2, %1, %0" : "+v"(d) : "a"(x), "v"(w));
        else                 asm volatile("v_mfma_f32_16x16x32_bf16 %0, %1, %2, %0" : "+v"(d) : "a"(x), "v"(w));
    }
}
DI void mv0(f32x4& d, const u32x4& a, const u32x4& b) { asm volatile("v_mfma_f32_16x16x32_bf16 %0, %1, %2, 0" : "=&v"(d) : "v"(a), "v"(b)); }
DI void mva(f32x4& d, const u32x4& a, const u32x4& b) { asm volatile("v_mfma_f32_16x16x32_bf16 %0, %1, %2, %0" : "+v"(d) : "v"(a), "v"(b)); }
DI void mvc(f32x4& d, const u32x4& a, const u32x4& b, const f32x4& c) { asm volatile("v_mfma_f32_16x16x32_bf16 %0, %1, %2, %3" : "=&v"(d) : "v"(a), "v"(b), "v"(c)); }
#define PRE_MFMA()  do { SB(); asm volatile("s_nop 1"); SB(); } while (0)
#define POST_MFMA() do { SB(); asm volatile("s_nop 7"); SB(); } while (0)

DI u32 pk2(float a, float b) { f32x2 v = {a, b}; return __builtin_bit_cast(u32, __builtin_convertvector(v, bf16x2)); }
DI u32x2 pk4(f32x4 v) { u32x2 r = {pk2(v[0], v[1]), pk2(v[2], v[3])}; return r; }
DI u32x4 cat(u32x2 a, u32x2 b) { u32x4 r = {a[0], a[1], b[0], b[1]}; return r; }
DI u32x2 trread(u32 addr) {
    return __builtin_bit_cast(u32x2, __builtin_amdgcn_ds_read_tr16_b64_v4i16((LAS s16x4*)(size_t)addr)); }
template <class T> DI T lds_ld(u32 addr) { return *(LAS T*)(size_t)addr; }
template <class T> DI void lds_st(u32 addr, T v) { *(LAS T*)(size_t)addr = v; }
DI float vmax(float a, float b) { return __builtin_elementwise_maximum(a, b); }
DI float sel4(int i, float a, float b, float c, float d) { float r = a; r = i == 1 ? b : r; r = i == 2 ? c : r; r = i == 3 ? d : r; return r; }
DI u32 umax(u32 a, u32 b) { return __builtin_elementwise_max(a, b); }
DI u32 umax4(u32 m, u32x4 v) { return umax(umax(umax(umax(m, v[0]), v[1]), v[2]), v[3]); }
DI void swap16(float& a, float& b) { auto r = __builtin_amdgcn_permlane16_swap(__float_as_uint(a), __float_as_uint(b), false, false); a = __uint_as_float(r[0]); b = __uint_as_float(r[1]); }
DI void swap32(float& a, float& b) { auto r = __builtin_amdgcn_permlane32_swap(__float_as_uint(a), __float_as_uint(b), false, false); a = __uint_as_float(r[0]); b = __uint_as_float(r[1]); }
DI rsrc_t mk_rsrc(const void* p, u32 bytes) { return __builtin_amdgcn_make_buffer_rsrc((void*)p, (short)0, (int)bytes, 0x00020000); }
DI u64 mix(u64 e) { return e * 0x9E3779B97F4A7C15ull + 0x632BE59BD9B4E019ull; }

template <int OFF> DI void ld128_sc1(u32x4& d, u32 voff, rsrc_t rs, u32 soff) {
    asm volatile("buffer_load_dwordx4 %0, %1, %2, %3 offen offset:%4 sc1" : "=v"(d) : "v"(voff), "s"(rs), "s"(soff), "i"(OFF));
}
template <int OFF> DI void rld128_sc1(u32x4& d, u32 voff, rsrc_t rs, u32 soff) {
    asm volatile("buffer_load_dwordx4 %0, %1, %2, %3 offen offset:%4 sc1" : "+v"(d) : "v"(voff), "s"(rs), "s"(soff), "i"(OFF));
}
DI void ld64_sc1(u32x2& d, u32 voff, rsrc_t rs, u32 soff) {
    asm volatile("buffer_load_dwordx2 %0, %1, %2, %3 offen sc1" : "=v"(d) : "v"(voff), "s"(rs), "s"(soff));
}
template <int OFF> DI void st128_sc1(u32x4 v, u32 voff, rsrc_t rs, u32 soff) {
    asm volatile("buffer_store_dwordx4 %0, %1, %2, %3 offen offset:%4 sc1\n\ts_nop 1" :: "v"(v), "v"(voff), "s"(rs), "s"(soff), "i"(OFF) : "memory");
}
template <int OFF> DI void st64_sc1(u32x2 v, u32 voff, rsrc_t rs, u32 soff) {
    asm volatile("buffer_store_dwordx2 %0, %1, %2, %3 offen offset:%4 sc1" :: "v"(v), "v"(voff), "s"(rs), "s"(soff), "i"(OFF) : "memory");
}
template <int LDSOFF> DI void set_m0(u32 base) { asm volatile("s_add_u32 m0, %0, %1" :: "s"(base), "i"(LDSOFF) : "scc"); }
DI void glds_go(u32 voff, rsrc_t rs, u32 soff) {
    asm volatile("buffer_load_dwordx4 %0, %1, %2 offen lds" :: "v"(voff), "s"(rs), "s"(soff)); }
template <int LDSOFF> DI void glds(u32 base, u32 voff, rsrc_t rs, u32 soff) {
    asm volatile("s_add_u32 m0, %0, %4\n\ts_nop 0\n\tbuffer_load_dwordx4 %1, %2, %3 offen lds"
                 :: "s"(base), "v"(voff), "s"(rs), "s"(soff), "i"(LDSOFF) : "scc");
}
template <int N> DI void wait_vm() { asm volatile("s_waitcnt vmcnt(%0)" :: "i"(N) : "memory"); }
DI void pin(u32x4& a) { asm volatile("" : "+v"(a)); }
DI void pin(u32x2& a) { asm volatile("" : "+v"(a)); }
DI void pin(u32& a) { asm volatile("" : "+v"(a)); }
DI bool all_eq(u32x2 v, u32x2 tag) { return __builtin_amdgcn_ballot_w64((v[0] != tag[0]) | (v[1] != tag[1])) == 0; }

DI void poll(rsrc_t ws, u32 voff, u32x2 tag) {
#pragma unroll 1
    for (int it = 0; it < (1 << 18); ++it) {
        u32x2 v;
        asm volatile("buffer_load_dwordx2 %0, %1, %2, 0 offen sc1\n\ts_waitcnt vmcnt(0)" : "=v"(v) : "v"(voff), "s"(ws) : "memory");
        if (all_eq(v, tag)) break;
        __builtin_amdgcn_s_sleep(1);
    }
}

#define TS(i) do {} while (0)

DI u32 phi2(float a, float b) {
    f32x2 t = {a, b};
    f32x2 y = t * 1.4426950408889634f;
    f32x2 u = t + 1.0f;
    f32x2 r = {vmax(u[0], __builtin_amdgcn_fmed3f(__builtin_amdgcn_exp2f(y[0]), 0.f, 1.f)),
               vmax(u[1], __builtin_amdgcn_fmed3f(__builtin_amdgcn_exp2f(y[1]), 0.f, 1.f))};
    r = r + 1e-6f;
    return pk2(r[0], r[1]);
}

template <bool ISA>
DI void body(const u16* __restrict__ xg, const u16* __restrict__ wg_, const u16* __restrict__ wog, u16* __restrict__ outg,
             char* wsp, int t, int g, char* smem) {
    constexpr const Sched& SC = ISA ? SCHED_A : SCHED_B;
    const int tid = threadIdx.x;
    const int lane = tid & 63;
    const int w = __builtin_amdgcn_readfirstlane(tid >> 6);
    const int gam = lane >> 4, c16 = lane & 15, hc = c16 >> 2;
    const int b = ISA ? 0 : 1;
    const int wg = (b * 16 + t) * 8 + g;
    const u32 lds0 = (u32)(size_t)(LAS char*)smem;
    TS(0);

    const rsrc_t rsW = mk_rsrc(wg_, DM * 3 * DM * 2);
    const rsrc_t rsX = mk_rsrc(xg + (size_t)(t * 128 + w * 32) * DM, 32 * DM * 2);
    const int rot = (t + g) % 5;
    u32 rck[5];
#pragma unroll
    for (int j = 0; j < 5; ++j) { int c = j + rot; c = c >= 5 ? c - 5 : c; rck[j] = (u32)c; }
    const u32 ldsw = lds0 + (u32)w * 5120u;
    const u32 ldsx = lds0 + XST_OFF + (u32)w * 20480u;
    u32 vx[4];
#pragma unroll
    for (int j = 0; j < 4; ++j) { int i = 8 * (j & 1) + (lane >> 3); vx[j] = (u32)((2 * i + (j >> 1)) * 1280 + (((lane & 7) ^ (i >> 1)) * 16)); }
    auto soffX = [&](auto H) { return rck[H.v >> 1] * 256u + (u32)(H.v & 1) * 128u; };
    auto issueX = [&](auto H) { const u32 so = soffX(H); sfor<0, 4>([&](auto J) { glds<(H.v % 5) * 4096 + J.v * 1024>(ldsx, vx[J.v], rsX, so); }); };
    issueX(IC<0>{});
    SB();
    u32 vw[5];
#pragma unroll
    for (int i = 0; i < 5; ++i) {
        int c = i * 64 + lane;
        int rl = c / 10, ch = c - rl * 10;
        vw[i] = (u32)((rl ^ ((rl >> 1) & 4)) * 3840 + ch * 16);
    }
    const u32 wbase = (u32)w * (32u * 3840u) + (u32)g * 160u;
    auto soffW = [&](auto S) { return rck[st_chunk(S.v)] * (128u * 3840u) + (u32)(st_type(S.v) * 1280) + wbase; };
    auto issueW = [&](auto S) { const u32 so = soffW(S); sfor<0, 5>([&](auto I) { glds<st_slot(S.v) * SLOT_B + I.v * 1024>(ldsw, vw[I.v], rsW, so); }); };
    issueW(IC<0>{});
    issueX(IC<1>{});
    const rsrc_t rsWo = mk_rsrc(wog, DM * DM * 2);
    const rsrc_t rsWS = mk_rsrc(wsp, WS_BYTES);
    const rsrc_t rsO = mk_rsrc(outg + (size_t)(t * 128 + w * 32) * (2 * DM) + b * DM + g * 80, 31 * 2 * DM * 2 + 160);
    u32 cpf;
    {
        u64 pc; u32 len;
        if constexpr (ISA) asm volatile("s_getpc_b64 %0\n.Lfk_pc_a:\n\ts_mov_b32 %1, .Lfk_end_a-.Lfk_pc_a" : "=s"(pc), "=s"(len));
        else               asm volatile("s_getpc_b64 %0\n.Lfk_pc_b:\n\ts_mov_b32 %1, .Lfk_end_b-.Lfk_pc_b" : "=s"(pc), "=s"(len));
        len = len > 65536u ? 0u : len;
        const rsrc_t rsC = mk_rsrc((const void*)pc, len);
        asm volatile("buffer_load_dword %0, %1, %2, 0 offen" : "=v"(cpf) : "v"(lane < 16 ? (u32)((g & 3) + 4 * (lane + 16 * w)) * 128u : OOB), "s"(rsC));
    }
    issueW(IC<1>{});
    u32x2 ep0, ep1; u32 otouch, wtouch;
    {
        u32 o0 = ISA ? (u32)(128 * (1 + t)) : 0u;
        u32 o1 = ISA ? (u32)(128 * t) : 0u;
        u32 z = 0;
        ld64_sc1(ep0, z, rsWS, o0);
        ld64_sc1(ep1, z, rsWS, o1);
        asm volatile("buffer_load_dword %0, %1, %2, 0 offen" : "=v"(otouch) : "v"(z), "s"(rsO));
        const u32 tv = lane == 0 ? WS_O + (u32)((b * 16 + t) * 4 + w) * OBLK + 40960u : lane == 1 ? WS_P1 + (u32)wg * 8192u + 8064u : lane == 2 ? WS_O - 128u : OOB;
        asm volatile("buffer_load_dword %0, %1, %2, 0 offen" : "=v"(wtouch) : "v"(tv), "s"(rsWS));
    }
    SB();

    u32 vo[5], vo4[5];
#pragma unroll
    for (int i = 0; i < 5; ++i) {
        int c = i * 64 + lane;
        int rl = c / 10, ch = c - rl * 10;
        int k = rl ^ ((rl >> 1) & 4);
        int ga = k >> 3, e = k & 7;
        vo[i] = (u32)((20 * (e >> 2) + 4 * ga + (e & 3)) * 1280 + ch * 16);
        vo4[i] = (u32)((80 * (e >> 2) + 20 * ga + 16 + (e & 3)) * 1280 + ch * 16);
        asm volatile("" : "+v"(vo[i]), "+v"(vo4[i]));
    }
    auto soffO = [&](auto S) {
        constexpr int ck = st_chunk(S.v);
        const u32 row = ck < 4 ? 80u * (u32)((g + 2 * ck + (w >> 1)) & 7) + 40u * (u32)(w & 1) : 160u * (u32)(((g >> 1) + w) & 3);
        return row * 1280u + (u32)g * 160u;
    };

    u32 rb[2][2][2];
    {
        int q = (lane >> 2) & 3, p = lane & 3;
#pragma unroll
        for (int tt = 0; tt < 2; ++tt) {
            int row = 8 * gam + 4 * (tt ^ (gam & 1)) + q;
            rb[0][tt][0] = lds0 + (u32)(row * 160 + 8 * p);
            rb[0][tt][1] = lds0 + (u32)(row * 160 + 40 * p + 32);
            rb[1][tt][0] = rb[0][tt][0] + 3 * SLOT_B;
            rb[1][tt][1] = rb[0][tt][1] + 3 * SLOT_B;
            asm volatile("" : "+v"(rb[0][tt][0]), "+v"(rb[0][tt][1]), "+v"(rb[1][tt][0]), "+v"(rb[1][tt][1]));
        }
    }
    u32x2 Bh[2][5][2];
    auto readB = [&](auto N, auto F, auto T) {
        constexpr int n = N.v, s = n >> 2, kk = n & 3, slot = st_slot(s), hf = slot / 3, f = F.v;
        constexpr int off = (slot % 3) * SLOT_B + kk * 5120;
        Bh[n & 1][f][T.v] = f < 4 ? trread(rb[hf][T.v][0] + off + 40 * f) : trread(rb[hf][T.v][1] + off);
    };
    auto readBall = [&](auto N) { sfor<0, 5>([&](auto F) { readB(N, F, IC<0>{}); readB(N, F, IC<1>{}); SB(); }); };
    u32 rx[2];
#pragma unroll
    for (int kk = 0; kk < 2; ++kk) { rx[kk] = ldsx + (u32)(c16 * 128 + (((4 * kk + gam) ^ (c16 >> 1)) * 16)); asm volatile("" : "+v"(rx[kk])); }
    u32x4 xf[2][20];
    auto readX = [&](auto H, auto J) {
        constexpr int h = H.v, p = J.v & 1, kk = J.v >> 1;
        xf[p][h * 2 + kk] = lds_ld<u32x4>(rx[kk] + (h % 5) * 4096 + p * 2048);
    };

    f32x4 acc[2][5];
    const u32 xch = lds0 + XCH_OFF;
    u32 vl = (u32)lane * 16u;
    const u32 myP1 = WS_P1 + (u32)wg * 8192u;
    const u32 obase = WS_O + (u32)((b * 16 + t) * 4 + w) * OBLK;
    u32 vH = vl + (u32)(g & 1) * 8u;
    u32 vflag = lane == 0 ? 0u : OOB;
    u32 vh0 = (ISA && w == 3 && lane >= 32) ? OOB : vl;
    u32 vh1 = (ISA && w == 3) ? OOB : vl;
    u32 f1src = ISA ? WS_F1 + (u32)((t > 0 ? wg - 8 : wg) * 4 + w) * 128u : WS_F1 + (u32)((128 + g) * 4 + w) * 128u + (u32)c16 * 4096u;
    u32 f2src = WS_F2 + (u32)(((b * 16 + t) * 8 + (lane & 7)) * 4 + w) * 128u;
    u32 vst0 = xch + (u32)(w * 5120 + c16 * 320 + gam * 8), vst1 = xch + (u32)(w * 5120 + c16 * 320 + gam * 40 + 32);
    u32 vout[5];
#pragma unroll
    for (int i = 0; i < 5; ++i) { int c = i * 64 + lane; int rl = c / 10; vout[i] = (u32)(rl * 2560 + (c - rl * 10) * 16); asm volatile("" : "+v"(vout[i])); }
    u32x4 on = {0x3F803F80u, 0x3F803F80u, 0x3F803F80u, 0x3F803F80u};
    asm volatile("" : "+v"(vl), "+v"(vH), "+v"(vflag), "+v"(vh0), "+v"(vh1), "+v"(f1src), "+v"(f2src), "+v"(vst0), "+v"(vst1), "+v"(on));
    u32x2 tag, tagp;
    u32 wpf, vwpf = lane < 50 ? (u32)((((t & 3) * 8 + g) * 4 + w) * 50 + lane) * 128u : OOB;
    asm volatile("" : "+v"(vwpf));

    f32x4 cown, cprv; u32x2 mown, mprv;
    u32 kwr, vwr, krd, vrd;
    u32x4 S1, S2;
    u32 a1m[4], a2m[4], a2n[4], a3m[4], l8, zrd;
    if constexpr (ISA) {
        const bool hasprev = (w > 0) || (t > 0);
        bool vo_[4], vp_[4];
#pragma unroll
        for (int j = 0; j < 4; ++j) {
            int kk = 4 * gam + j;
            vo_[j] = (kk <= c16) && (kk >= c16 - 7);
            vp_[j] = (kk >= c16 + 9) && hasprev;
            cown[j] = vo_[j] ? 0.f : -1e30f;
            cprv[j] = vp_[j] ? 0.f : -1e30f;
        }
        mown[0] = (vo_[0] ? 0xFFFFu : 0u) | (vo_[1] ? 0xFFFF0000u : 0u);
        mown[1] = (vo_[2] ? 0xFFFFu : 0u) | (vo_[3] ? 0xFFFF0000u : 0u);
        mprv[0] = (vp_[0] ? 0xFFFFu : 0u) | (vp_[1] ? 0xFFFF0000u : 0u);
        mprv[1] = (vp_[2] ? 0xFFFFu : 0u) | (vp_[3] ? 0xFFFF0000u : 0u);
        kwr = xch + (c16 >= 8 ? (u32)(w + 1) * 8192u : 0u) + (u32)(gam * 8 + (c16 & 7)) * 16u;
        vwr = xch + (gam >= 2 ? (u32)(w + 1) * 8192u : 0u) + 4096u + (u32)(lane & 31) * 8u;
        krd = xch + (u32)w * 8192u + (u32)(gam * 8 + (c16 & 7)) * 16u;
        vrd = xch + (u32)w * 8192u + 4096u + (u32)(lane & 31) * 8u;
        asm volatile("" : "+v"(cown), "+v"(cprv), "+v"(mown), "+v"(mprv), "+v"(kwr), "+v"(vwr), "+v"(krd), "+v"(vrd));
    } else {
        const u32 one = (c16 & 1) ? 0x3F800000u : 0x00003F80u;
        const u32 a0 = (gam == hc && !(c16 & 2)) ? one : 0u, a1 = (gam == hc && (c16 & 2)) ? one : 0u;
        S1 = (u32x4){a0, a1, 0u, 0u}; S2 = (u32x4){0u, 0u, a0, a1};
        l8 = xch + 32768u + (u32)lane * 8u; zrd = xch + 32768u + (u32)(6 * 512) + (u32)gam * 128u;
        const u32 zb = xch + 32768u + 4096u;
#pragma unroll
        for (int h = 0; h < 4; ++h) {
            a1m[h] = gam == h ? l8 + 4 * 512 : zb;
            a2m[h] = hc == h ? l8 + 5 * 512 : zb;
            a2n[h] = (hc == h && gam == h) ? l8 + 7 * 512 : zb;
            a3m[h] = gam == h ? xch + 32768u + (u32)(7 * 512 + h * 128 + ((h + 1) & 3) * 32) : zb;
            asm volatile("" : "+v"(a1m[h]), "+v"(a2m[h]), "+v"(a2n[h]), "+v"(a3m[h]));
        }
        asm volatile("" : "+v"(S1), "+v"(S2), "+v"(l8), "+v"(zrd));
    }

    u32x4 Kown[2][4], Kprv[2][4], Vop[2][5];
    u32x4 PK[5];
    u32x4 hcp[2];
    u32x2 flg;
    u32x4 gl[16];

    auto filler = [&](auto M) {
        constexpr int m = M.v;
        if constexpr (m == M_WPF) asm volatile("buffer_load_dword %0, %1, %2, 0 offen" : "=v"(wpf) : "v"(vwpf), "s"(rsWo));
        if constexpr (ISA) {
            if constexpr (m == M_PUBA) {
                hcp[0] = lds_ld<u32x4>(xch + 4 * 8192 + (u32)w * 2048u + vl);
                hcp[1] = lds_ld<u32x4>(xch + 4 * 8192 + (u32)w * 2048u + vl + 1024);
            }
            if constexpr (m == M_PUBA + 4) st128_sc1<0>(hcp[0], vh0, rsWS, myP1 + (u32)w * 2048u);
            if constexpr (m == M_PUBA + 5) st128_sc1<1024>(hcp[1], vh1, rsWS, myP1 + (u32)w * 2048u);
            if constexpr (m >= 295) {
                constexpr int i = m - 295;
                if constexpr (i < 4) { Kprv[i >> 1][(i & 1) * 2] = lds_ld<u32x4>(krd + 2 * i * 512); Kprv[i >> 1][(i & 1) * 2 + 1] = lds_ld<u32x4>(krd + (2 * i + 1) * 512); }
#pragma unroll
                for (int q = 2 * i; q < 2 * i + 2; ++q) { u32x2 v = lds_ld<u32x2>(vrd + q * 256); Vop[q / 5][q % 5][2] = v[0]; Vop[q / 5][q % 5][3] = v[1]; }
            }
        } else {
            if constexpr (m >= M_CHK && m < M_CHK + 16) ld128_sc1<0>(gl[m - M_CHK], vl, rsWS, WS_P1 + (u32)((16 + ((m - M_CHK + 4 * t) & 15)) * 8 + g) * 8192u + (u32)w * 1024u);
        }
        if constexpr (m == M_POLL) ld64_sc1(flg, f1src, rsWS, 0u);
    };

    auto sync = [&](auto S, auto LG) {
        constexpr int s = S.v;
        static_assert(SC.n_sync[s] >= 0, "W piece not issued yet");
        constexpr int xc = sync_m(s) % 20 == 15 ? xrd_half(sync_m(s) / 5) : -1;
        constexpr int nw = (xc >= 0 && SC.n_x[xc < 0 ? 0 : xc] < SC.n_sync[s]) ? SC.n_x[xc < 0 ? 0 : xc] : SC.n_sync[s];
        SB();
        if constexpr (LG.v) asm volatile("s_waitcnt vmcnt(%0) lgkmcnt(0)\n\ts_barrier" :: "i"(cap63(nw)) : "memory");
        else asm volatile("s_waitcnt vmcnt(%0)\n\ts_barrier" :: "i"(cap63(nw)) : "memory");
        SB();
    };
    auto group = [&](auto M) {
        constexpr int m = M.v, n = m / 5, f = m % 5, s = n >> 2, kk = n & 3, ty = st_type(s), ck = st_chunk(s);
        constexpr bool trans = ty == 0 || (ISA && ty == 1);
        constexpr bool first = ck == 0 && kk == 0;
        constexpr bool pre = (n + 1) % 20 != 0;
        constexpr int xc = xrd_half(n);
        static_assert(xc < 0 || SC.n_x[xc < 0 ? 0 : xc] >= 0, "x piece not issued yet");
        if constexpr (m == sync_m(s)) sync(IC<s>{}, IC<(ISA && s == 14)>{});
        else if constexpr (f == 0 && xc >= 0) { wait_vm<cap63(SC.n_x[xc < 0 ? 0 : xc])>(); SB(); }
        constexpr int vk = SC.kind[m], vi = SC.id[m], vp = SC.piece[m];
        const u32x4 Bw = cat(Bh[n & 1][f][0], Bh[n & 1][f][1]);
        if constexpr (vk == 1) set_m0<st_slot(vi) * SLOT_B + vp * 1024>(ldsw);
        if constexpr (vk == 2) set_m0<(vi % 5) * 4096 + vp * 1024>(ldsx);
        mml<trans, first>(acc[0][f], xf[0][ck * 4 + kk], Bw);
        SB();
        if constexpr (pre) readB(IC<n + 1>{}, IC<f>{}, IC<0>{});
        if constexpr (vk == 1) {
            if constexpr (st_type(vi) == 3) glds_go(st_chunk(vi) < 4 ? vo[vp] : vo4[vp], rsWo, soffO(IC<vi>{}));
            else glds_go(vw[vp], rsW, soffW(IC<vi>{}));
        }
        if constexpr (vk == 2) glds_go(vx[vp], rsX, soffX(IC<vi>{}));
        if constexpr (xc >= 0 && f < 4) readX(IC<xc>{}, IC<f>{});
        filler(M);
        SB();
        mml<trans, first>(acc[1][f], xf[1][ck * 4 + kk], Bw);
        SB();
        if constexpr (pre) readB(IC<n + 1>{}, IC<f>{}, IC<1>{});
        SB();
    };

    SB();
    wait_vm<SC.n_init>();
    sfor<0, 4>([&](auto J) { readX(IC<0>{}, J); });
    asm volatile("s_barrier" ::: "memory");
    TS(1);
    readBall(IC<0>{});

    sfor<0, 100>(group);
    POST_MFMA();
    {
        pin(ep0); pin(ep1); pin(otouch); pin(wtouch); pin(cpf);
        u64 a = mix(((u64)(u32)__builtin_amdgcn_readfirstlane(ep0[1]) << 32) | (u32)__builtin_amdgcn_readfirstlane(ep0[0]));
        u64 c = mix(((u64)(u32)__builtin_amdgcn_readfirstlane(ep1[1]) << 32) | (u32)__builtin_amdgcn_readfirstlane(ep1[0]));
        tag = (u32x2){(u32)a, (u32)(a >> 32)}; tagp = (u32x2){(u32)c, (u32)(c >> 32)};
        pin(tag);
    }
    if constexpr (ISA) {
#pragma unroll
        for (int p = 0; p < 2; ++p)
#pragma unroll
            for (int h = 0; h < 4; ++h) {
                f32x4 k4 = acc[p][4]; asm volatile("" : "+v"(k4));
                Kown[p][h] = cat(pk4(acc[p][h]), pk4(k4)); pin(Kown[p][h]);
            }
    } else {
#pragma unroll
        for (int f = 0; f < 5; ++f) {
            PK[f] = (u32x4){phi2(acc[0][f][0], acc[0][f][1]), phi2(acc[0][f][2], acc[0][f][3]), phi2(acc[1][f][0], acc[1][f][1]), phi2(acc[1][f][2], acc[1][f][3])};
            pin(PK[f]);
        }
    }
    sync(IC<4>{}, IC<0>{});
    readBall(IC<20>{});
    if constexpr (ISA) {
#pragma unroll
        for (int i = 0; i < 8; ++i) lds_st(kwr + i * 512, Kown[i >> 2][i & 3]);
        SB();
    }
    sfor<100, 200>(group);
    POST_MFMA();
    TS(2);
    if constexpr (ISA) {
#pragma unroll
        for (int i = 0; i < 10; ++i) {
            u32x2 v = pk4(acc[i / 5][i % 5]);
            Vop[i / 5][i % 5][0] = v[0]; Vop[i / 5][i % 5][1] = v[1];
            lds_st(vwr + i * 256, v);
        }
        sync(IC<9>{}, IC<1>{});
        readBall(IC<40>{});
    } else {
        u32x4 VV[5];
        f32x4 G[8], d1[4], d2[4], zz[4], d3, z4;
        VV[4] = cat(pk4(acc[0][4]), pk4(acc[1][4]));
        PRE_MFMA();
        sfor<0, 4>([&](auto H) {
            constexpr int h = H.v;
            mv0(zz[h], PK[h], on);
            SB();
            VV[h][0] = pk2(acc[0][h][0], acc[0][h][1]); VV[h][1] = pk2(acc[0][h][2], acc[0][h][3]);
            SB();
            mv0(d2[h], PK[h], VV[4]);
            SB();
            VV[h][2] = pk2(acc[1][h][0], acc[1][h][1]); VV[h][3] = pk2(acc[1][h][2], acc[1][h][3]);
            SB();
        });
        mv0(d3, PK[4], VV[4]);
        mv0(z4, PK[4], on);
        SB();
        sfor<0, 4>([&](auto H) {
            constexpr int h = H.v;
            mv0(d1[h], PK[4], VV[h]);
            SB();
            G[6][h] = sel4(hc, zz[0][h], zz[1][h], zz[2][h], zz[3][h]);
            SB();
            mv0(G[h], PK[h], VV[h]);
            SB();
            G[5][h] = sel4(hc, d2[0][h], d2[1][h], d2[2][h], d2[3][h]);
            SB();
        });
#pragma unroll
        for (int j = 0; j < 4; ++j) G[7][j] = gam == hc ? d3[j] : z4[j];
        const u32 wr = xch + (u32)w * 8192u + vl;
        lds_st(wr + 5 * 1024, G[5]); lds_st(wr + 6 * 1024, G[6]); lds_st(wr + 7 * 1024, G[7]);
        lds_st(l8 + 4096u, (u32x2){0u, 0u});
        POST_MFMA();
#pragma unroll
        for (int i = 0; i < 4; ++i) lds_st(wr + i * 1024, G[i]);
#pragma unroll
        for (int j = 0; j < 4; ++j) G[4][j] = sel4(gam, d1[0][j], d1[1][j], d1[2][j], d1[3][j]);
        lds_st(wr + 4 * 1024, G[4]);
        SB();
        asm volatile("s_waitcnt vmcnt(%0) lgkmcnt(0)\n\ts_barrier" :: "i"(cap63(SC.n_sync[9] - 1)) : "memory");
        SB();
        f32x4 s[2];
#pragma unroll
        for (int i = 0; i < 2; ++i) {
            const u32 rd = xch + (u32)w * 2048u + vl + i * 1024;
            s[i] = (lds_ld<f32x4>(rd) + lds_ld<f32x4>(rd + 8192)) + (lds_ld<f32x4>(rd + 16384) + lds_ld<f32x4>(rd + 24576));
        }
        sfor<0, 5>([&](auto F) { readB(IC<40>{}, F, IC<0>{}); readB(IC<40>{}, F, IC<1>{}); });
        st128_sc1<0>(cat(pk4(s[0]), pk4(s[1])), vl, rsWS, myP1 + (u32)w * 1024u);
        SB();
    }
    TS(3);

    sfor<200, M_FLAG>(group);
    wait_vm<SC.pos[M_FLAG] - 1 - SC.pos[ISA ? M_PUBA + 6 : 200]>();
    st64_sc1<0>(tag, vflag, rsWS, WS_F1 + (u32)(wg * 4 + w) * 128u);
    SB();
    sfor<M_FLAG, M_CHK>(group);
    TS(4);
    {
        wait_vm<SC.pos[M_CHK] - xpre(ISA, M_CHK) - SC.pos[M_POLL + 1]>();
        pin(flg); pin(wpf);
        if constexpr (ISA) {
            if (t > 0 && !all_eq(flg, tagp)) poll(rsWS, f1src, tagp);
            SB();
            const u32 so = t > 0 ? myP1 - 8u * 8192u + (u32)w * 2048u : OOB;
            ld128_sc1<0>(hcp[0], vh0, rsWS, so);
            ld128_sc1<1024>(hcp[1], vh1, rsWS, so);
        } else {
            if (!all_eq(flg, tag)) poll(rsWS, f1src, tag);
        }
        SB();
    }
    TS(5);
    sfor<M_CHK, M_PARK>(group);
    if constexpr (ISA) {
        wait_vm<SC.pos[M_PARK] - SC.pos[M_CHK]>();
        pin(hcp[0]); pin(hcp[1]);
        lds_st(xch + (u32)w * 2048u + vl, hcp[0]);
        lds_st(xch + (u32)w * 2048u + vl + 1024, hcp[1]);
        SB();
    }
    sfor<M_PARK, 300>(group);
    POST_MFMA();
    TS(6);

    u32x4 of[2][20];
    auto publish = [&](auto P, u32x2 h4) {
        constexpr int p = P.v;
        st128_sc1<0>(of[p][0], vl, rsWS, obase + (u32)p * 20480u + (u32)g * 2048u);
        st128_sc1<1024>(of[p][1], vl, rsWS, obase + (u32)p * 20480u + (u32)g * 2048u);
        st64_sc1<0>(h4, vH, rsWS, obase + (u32)p * 20480u + 16384u + (u32)(g >> 1) * 1024u);
    };
    if constexpr (ISA) {
        u32x4 Qf[2][4];
        f32x4 so[2][4], sp[2][4];
        sfor<0, 2>([&](auto P) {
            constexpr int p = P.v;
            u32x2 q4 = pk4(acc[p][4]);
            sfor<0, 4>([&](auto H) {
                constexpr int h = H.v, i = p * 4 + h;
                Qf[p][h] = cat(pk4(acc[p][h]), (u32x2){gam == h ? q4[0] : 0u, gam == h ? q4[1] : 0u});
                SB();
                if constexpr (i > 0) {
                    mvc(so[(i - 1) >> 2][(i - 1) & 3], Kown[(i - 1) >> 2][(i - 1) & 3], Qf[(i - 1) >> 2][(i - 1) & 3], cown);
                    mvc(sp[(i - 1) >> 2][(i - 1) & 3], Kprv[(i - 1) >> 2][(i - 1) & 3], Qf[(i - 1) >> 2][(i - 1) & 3], cprv);
                    SB();
                }
            });
        });
        mvc(so[1][3], Kown[1][3], Qf[1][3], cown);
        mvc(sp[1][3], Kprv[1][3], Qf[1][3], cprv);
        SB();
        asm volatile("s_nop 2" :: "v"(cown), "v"(cprv));
        SB();
        const float cs = 0.22360679774997896f * 1.4426950408889634f;
        f32x4 oh[2][4], o4[2][4], dn[2][4];
        u32x4 Pf[2][4];
        auto pv = [&](auto P, auto I) {
            constexpr int p = P.v, h = I.v / 3, k = I.v % 3;
            SB();
            if constexpr (k == 0) mv0(dn[p][h], on, Pf[p][h]); else if constexpr (k == 1) mv0(oh[p][h], Vop[p][h], Pf[p][h]); else mv0(o4[p][h], Vop[p][4], Pf[p][h]);
            SB();
        };
        sfor<0, 2>([&](auto P) {
            constexpr int p = P.v;
            f32x4 L[4]; float mx[4];
            sfor<0, 4>([&](auto H) {
                constexpr int h = H.v;
#pragma unroll
                for (int j = 0; j < 4; ++j) L[h][j] = vmax(so[p][h][j], sp[p][h][j]);
                mx[h] = vmax(vmax(vmax(L[h][0], L[h][1]), L[h][2]), L[h][3]);
                if constexpr (p == 1) pv(IC<0>{}, IC<h>{});
            });
            swap16(mx[0], mx[1]); swap16(mx[2], mx[3]);
            float xa = vmax(mx[0], mx[1]), xb = vmax(mx[2], mx[3]);
            if constexpr (p == 1) pv(IC<0>{}, IC<4>{});
            swap32(xa, xb);
            float z0 = vmax(xa, xb) * -cs, z1 = z0;
            if constexpr (p == 1) pv(IC<0>{}, IC<5>{});
            swap32(z0, z1);
            float m0 = z0, m1 = z0, m2 = z1, m3 = z1;
            if constexpr (p == 1) pv(IC<0>{}, IC<6>{});
            swap16(m0, m1); swap16(m2, m3);
            const float mc[4] = {m0, m1, m2, m3};
            if constexpr (p == 1) pv(IC<0>{}, IC<7>{});
            sfor<0, 4>([&](auto H) {
                constexpr int h = H.v;
                u32 p0 = pk2(__builtin_amdgcn_exp2f(__builtin_fmaf(L[h][0], cs, mc[h])), __builtin_amdgcn_exp2f(__builtin_fmaf(L[h][1], cs, mc[h])));
                u32 p1 = pk2(__builtin_amdgcn_exp2f(__builtin_fmaf(L[h][2], cs, mc[h])), __builtin_amdgcn_exp2f(__builtin_fmaf(L[h][3], cs, mc[h])));
                Pf[p][h] = (u32x4){p0 & mown[0], p1 & mown[1], p0 & mprv[0], p1 & mprv[1]};
                if constexpr (p == 1) pv(IC<0>{}, IC<8 + h>{});
            });
            SB();
        });
        sfor<0, 2>([&](auto P) {
            constexpr int p = P.v;
            if constexpr (p == 1) POST_MFMA();
            float r[4]; u32x2 o2[4];
            sfor<0, 4>([&](auto H) {
                constexpr int h = H.v;
                if constexpr (p == 0) { pv(IC<1>{}, IC<2 * h>{}); }
                asm volatile("" : "+v"(dn[p][h]));
                r[h] = __builtin_amdgcn_rcpf(dn[p][h][0]);
                f32x4 t4 = {oh[p][h][0] * r[h], oh[p][h][1] * r[h], oh[p][h][2] * r[h], oh[p][h][3] * r[h]};
                if constexpr (p == 0) { pv(IC<1>{}, IC<2 * h + 1>{}); }
                o2[h] = pk4(t4);
            });
            f32x4 s4;
            sfor<0, 4>([&](auto J) {
                if constexpr (p == 0) { pv(IC<1>{}, IC<8 + J.v>{}); }
                s4[J.v] = sel4(gam, o4[p][0][J.v], o4[p][1][J.v], o4[p][2][J.v], o4[p][3][J.v]);
            });
            float r4 = sel4(gam, r[0], r[1], r[2], r[3]);
            of[p][0] = cat(o2[0], o2[1]); of[p][1] = cat(o2[2], o2[3]);
            publish(P, pk4(s4 * r4));
            SB();
        });
    } else {
        u32x4 Qf[2][4];
        f32x4 s1, s2;
        u32x2 q4[2];
        sfor<0, 10>([&](auto I) {
            constexpr int i = I.v, p = i / 5, f = (i % 5 + 4) % 5;
            if constexpr (i < 8) {
                wait_vm<cap63(SC.pos[NMG] - SC.pos[M_CHK + 2 * i + 2])>(); pin(gl[2 * i]); pin(gl[2 * i + 1]); SB();
                if constexpr (i == 0) { mv0(s1, S1, gl[0]); mv0(s2, S2, gl[0]); } else { mva(s1, S1, gl[2 * i]); mva(s2, S2, gl[2 * i]); }
                SB();
            }
            if constexpr (i == 9) {
                lds_st(l8 + (u32)w * 1024u, pk4(s1));
                lds_st(l8 + (u32)w * 1024u + 512u, pk4(s2));
                SB();
            }
            u32x2 v_ = {phi2(acc[p][f][0], acc[p][f][1]), phi2(acc[p][f][2], acc[p][f][3])}; pin(v_);
            if constexpr (f == 4) q4[p] = v_; else { Qf[p][f] = cat(v_, q4[p]); pin(Qf[p][f]); }
            SB();
            if constexpr (i < 8) { mva(s1, S1, gl[2 * i + 1]); mva(s2, S2, gl[2 * i + 1]); SB(); }
        });
        asm volatile("s_waitcnt lgkmcnt(0)\n\ts_barrier" ::: "memory");
        u32x4 A1[4], A2[4], A3[4];
#pragma unroll
        for (int h = 0; h < 4; ++h) {
            A3[h] = cat(lds_ld<u32x2>(zrd + h * 32), lds_ld<u32x2>(a3m[h]));
            A1[h] = cat(lds_ld<u32x2>(l8 + h * 512), lds_ld<u32x2>(a1m[h]));
            A2[h] = cat(lds_ld<u32x2>(a2m[h]), lds_ld<u32x2>(a2n[h]));
            SB();
        }
        f32x4 nh[2][4], n4[2], dn[2][4];
        auto nd = [&](auto P, auto I) {
            constexpr int p = P.v, h = I.v / 3, k = I.v % 3;
            SB();
            if constexpr (k == 0) mv0(dn[p][h], A3[h], Qf[p][h]); else if constexpr (k == 1) mv0(nh[p][h], A1[h], Qf[p][h]);
            else if constexpr (h == 0) mv0(n4[p], A2[0], Qf[p][0]); else mva(n4[p], A2[h], Qf[p][h]);
            SB();
        };
        sfor<0, 12>([&](auto I) { nd(IC<0>{}, I); });
        sfor<0, 2>([&](auto P) {
            constexpr int p = P.v;
            if constexpr (p == 1) POST_MFMA();
            float r[4]; u32x2 o2[4];
            sfor<0, 4>([&](auto H) {
                constexpr int h = H.v;
                if constexpr (p == 0) { nd(IC<1>{}, IC<3 * h>{}); }
                asm volatile("" : "+v"(dn[p][h]));
                r[h] = __builtin_amdgcn_rcpf(dn[p][h][0] + 1e-6f);
                if constexpr (p == 0) { nd(IC<1>{}, IC<3 * h + 1>{}); }
                f32x4 t4 = {nh[p][h][0] * r[h], nh[p][h][1] * r[h], nh[p][h][2] * r[h], nh[p][h][3] * r[h]};
                if constexpr (p == 0) { nd(IC<1>{}, IC<3 * h + 2>{}); }
                o2[h] = pk4(t4);
            });
            float r4 = sel4(gam, r[0], r[1], r[2], r[3]);
            of[p][0] = cat(o2[0], o2[1]); of[p][1] = cat(o2[2], o2[3]);
            publish(P, pk4(n4[p] * r4));
            SB();
        });
    }
    TS(7);
    sfor<15 + NOIN, 20>([&](auto S) {
        const u32 so = soffO(S);
        sfor<0, 5>([&](auto I) { glds<st_slot(S.v) * SLOT_B + I.v * 1024>(ldsw, st_chunk(S.v) < 4 ? vo[I.v] : vo4[I.v], rsWo, so); });
    });
    wait_vm<5 * (5 - NOIN)>();
    st64_sc1<0>(tag, vflag, rsWS, WS_F2 + (u32)(wg * 4 + w) * 128u);
    wait_vm<1>();
    asm volatile("s_barrier" ::: "memory");
    TS(8);
#define OAHEAD 5
    auto oload = [&](auto K, auto P) {
        constexpr int k = K.v;
        const u32 so = obase + (u32)P.v * 20480u + (k < 16 ? (u32)((g + k / 2) & 7) * 2048u : 16384u + (u32)(((g >> 1) + k - 16) & 3) * 1024u);
        ld128_sc1<(k < 16 ? (k & 1) * 1024 : 0)>(of[P.v][k], vl, rsWS, so);
    };
    auto kstep = [&](auto K) {
        constexpr int k = K.v, n = 60 + k;
        sfor<0, 5>([&](auto F) {
            constexpr int f = F.v;
            const u32x4 Bw = cat(Bh[n & 1][f][0], Bh[n & 1][f][1]);
            if constexpr (k == 0) mv0(acc[0][f], Bw, of[0][k]); else mva(acc[0][f], Bw, of[0][k]);
            SB();
            if constexpr (k < 19) readB(IC<n + 1>{}, F, IC<0>{});
            if constexpr (k >= 2 && k + OAHEAD < 20 && (f == 1 || f == 3)) oload(IC<k + OAHEAD>{}, IC<(f >> 1)>{});
            SB();
            if constexpr (k == 0) mv0(acc[1][f], Bw, of[1][k]); else mva(acc[1][f], Bw, of[1][k]);
            SB();
            if constexpr (k < 19) readB(IC<n + 1>{}, F, IC<1>{});
            SB();
        });
    };
    readBall(IC<60>{});
    sfor<0, 2>(kstep);
    poll(rsWS, f2src, tag);
    TS(9);
    sfor<2, 2 + OAHEAD>([&](auto K) { oload(K, IC<0>{}); oload(K, IC<1>{}); });
    SB();
    sfor<2, 20>([&](auto K) {
        constexpr int last = K.v + OAHEAD - 1 < 19 ? K.v + OAHEAD - 1 : 19;
        wait_vm<2 * (last - K.v)>(); pin(of[0][K.v]); pin(of[1][K.v]); SB();
        kstep(K);
    });
    POST_MFMA();
    TS(10);
#pragma unroll
    for (int p = 0; p < 2; ++p) {
#pragma unroll
        for (int f = 0; f < 4; ++f) lds_st(vst0 + (p * 160 + f * 40), pk4(acc[p][f]));
        lds_st(vst1 + p * 160, pk4(acc[p][4]));
    }
#pragma unroll
    for (int i = 0; i < 5; ++i)
        __builtin_amdgcn_raw_buffer_store_b128(lds_ld<u32x4>(xch + (u32)w * 5120u + vl + i * 1024), rsO, vout[i], 0, OAUX);
    if (g == 0 && w == 0) {
        if constexpr (ISA) {
            if (t > 0) st64_sc1<0>(tagp, vflag, rsWS, WS_EPOCH + (u32)(128 * t));
            if (t == 15) st64_sc1<0>(tag, vflag, rsWS, WS_EPOCH + (u32)(128 * 16));
        } else {
            if (t == 0) st64_sc1<0>(tag, vflag, rsWS, WS_EPOCH);
        }
    }
    TS(11);
    if constexpr (ISA) asm volatile(".Lfk_end_a:"); else asm volatile(".Lfk_end_b:");
}

__global__ __launch_bounds__(256, 1) void fused_kernel(const u16* __restrict__ xa, const u16* __restrict__ wa, const u16* __restrict__ woa,
                                                       const u16* __restrict__ xb, const u16* __restrict__ wb, const u16* __restrict__ wob,
                                                       u16* __restrict__ out, char* ws) {
    __shared__ __attribute__((aligned(16))) char smem[LDS_B];
    const int x = blockIdx.x & 7, idx = blockIdx.x >> 3;
    const int t = ((x >> 1) & 1) * 8 + (idx >> 2), g = (x & 1) * 4 + (idx & 3);
    const bool isa = x < 4;
    const u16* xp = isa ? xa : xb; const u16* wp = isa ? wa : wb; const u16* wop = isa ? woa : wob;
    asm volatile("" : "+s"(xp), "+s"(wp), "+s"(wop), "+s"(out), "+s"(ws));
    if (isa) body<true>(xp, wp, wop, out, ws, t, g, smem);
    else     body<false>(xp, wp, wop, out, ws, t, g, smem);
}

extern "C" void kernel_launch(void* const* d_in, const int* in_sizes, int n_in,
                              void* d_out, int out_size, void* d_ws, size_t ws_size,
                              hipStream_t stream) {
    fused_kernel<<<256, 256, 0, stream>>>((const u16*)d_in[0], (const u16*)d_in[1], (const u16*)d_in[2],
                                          (const u16*)d_in[3], (const u16*)d_in[4], (const u16*)d_in[5],
                                          (u16*)d_out, (char*)d_ws);
}
```

```cpp
#include <hip/hip_runtime.h>
#include <stdint.h>

typedef unsigned short u16;
typedef unsigned int u32;
typedef unsigned long long u64;
typedef __attribute__((ext_vector_type(4))) short s16x4;
typedef __attribute__((ext_vector_type(2))) __bf16 bf16x2;
typedef __attribute__((ext_vector_type(4))) float f32x4;
typedef __attribute__((ext_vector_type(2))) float f32x2;
typedef __attribute__((ext_vector_type(4))) u32 u32x4;
typedef __attribute__((ext_vector_type(2))) u32 u32x2;
typedef __amdgpu_buffer_rsrc_t rsrc_t;
#define LAS __attribute__((address_space(3)))
#define GAS __attribute__((address_space(1)))
#define DI __device__ __forceinline__
#define HD __host__ __device__
#define SB() __builtin_amdgcn_sched_barrier(0)

#define OAUX 2
#define XMAP 2

constexpr int DM = 640;
constexpr int SLOT_B = 20480;
constexpr int XST_OFF = 4 * SLOT_B;
constexpr int XCH_OFF = 6 * SLOT_B;
constexpr int LDS_B = 163840;
constexpr u32 OOB = 0x7FFFFF00u;

constexpr u32 WS_EPOCH = 0;
constexpr u32 WS_F1 = 4096;
constexpr u32 WS_F2 = WS_F1 + 256 * 4 * 128;
constexpr u32 WS_P1 = WS_F2 + 256 * 4 * 128;
constexpr u32 WS_O = WS_P1 + 256 * 8192;
constexpr u32 OBLK = 2 * 20480 + 128;
constexpr u32 WS_TS = WS_O + 2 * 16 * 4 * OBLK;
constexpr u32 WS_TS2 = WS_TS + 256 * 4 * 16 * 8;
constexpr u32 WS_BYTES = WS_TS2 + 256 * 4 * 64 * 4;

constexpr int NST = 20;
HD constexpr int st_type(int s)  { return s < 5 ? 1 : s < 10 ? 2 : s < 15 ? 0 : 3; }
HD constexpr int st_chunk(int s) { return s % 5; }
HD constexpr int st_slot(int s)  { return s < 8 ? s % 4 : (s - 4) % 6; }
HD constexpr int sync_m(int s) { return (s == 4 || s == 9) ? 20 * s + 20 : 20 * s + 15; }
HD constexpr int xrd_half(int n) { return (n & 1) && n < 19 ? (n + 1) / 2 : -1; }

constexpr int NMG = 300;
#ifndef M_WPF
#define M_WPF 130
#endif
constexpr int M_PUBA = 200;
#ifndef M_FLAG
#define M_FLAG 228
#define M_POLL 250
#define M_CHK 272
#endif
constexpr int M_PARK = 293;
#define LOOKA 50
#define OSPACE 4
#define SPV 2
#define SPQ0 5
#define SPQ 3
#define NOIN 3
struct Sched {
    int kind[NMG], id[NMG], piece[NMG];
    int pos[NMG + 1];
    int n_init, n_x[10], n_sync[NST];
    int left_stage;
};
HD constexpr int xpre(bool isa, int m) { return (!isa && m == 200) ? 1 : m == M_FLAG ? 1 : (isa && m == M_CHK) ? 2 : 0; }
HD constexpr int xop(bool isa, int m) {
    if (m == M_WPF) return 1;
    if (!isa && m >= M_CHK && m < M_CHK + 16) return 1;
    if (isa && m >= M_PUBA + 4 && m < M_PUBA + 6) return 1;
    if (m == M_POLL) return 1;
    return 0;
}
HD constexpr Sched make_sched(bool isa) {
    Sched r{};
    int uk[32] = {}, ui[32] = {}, ua[32] = {}, us[32] = {}; int nu = 0;
    auto add = [&](int k, int i, int a, int sp) { uk[nu] = k; ui[nu] = i; ua[nu] = a; us[nu] = sp; ++nu; };
    auto mx = [](int x, int y) { return x > y ? x : y; };
    auto xav = [&](int h) { return mx(h < 5 ? 0 : h == 5 ? 5 : 5 * (2 * (h - 5) + 1), 5 * (2 * h - 1) - LOOKA); };
    auto wfree = [](int q) { return q < 4 ? 0 : q < 8 ? sync_m(q - 3) : q < 10 ? sync_m(4) : sync_m(q - 5); };
    auto wav = [&](int q) { return mx(wfree(q), sync_m(q - 1) - LOOKA); };
    add(2, 3, xav(3), 1);
    for (int q = 2; q < 5; ++q) { add(1, q, wav(q), 1); add(2, 2 * q, xav(2 * q), 1); add(2, 2 * q + 1, xav(2 * q + 1), 1); }
    add(1, 5, wav(5), 1); add(1, 6, wav(6), 1);
    for (int q = 7; q < 10; ++q) add(1, q, wfree(q), SPV);
    add(1, 10, mx(wfree(10), M_WPF + 1), SPQ0); add(1, 11, wfree(11), SPQ0);
    for (int q = 12; q < 15; ++q) add(1, q, wfree(q), SPQ);
    for (int q = 15; q < 15 + NOIN; ++q) add(1, q, wfree(q), OSPACE);
    int endW[NST] = {}, endX[10] = {};
    int pos = 0;
    pos += 4; endX[0] = pos; pos += 5; endW[0] = pos; pos += 2; pos += 4; endX[1] = pos;
    pos += 5; endW[1] = pos; pos += 4; endX[2] = pos;
    r.n_init = pos - endW[0];
    int u = 0, pc = 0, nextok = 0;
    for (int m = 0; m < NMG; ++m) {
        pos += xpre(isa, m);
        r.pos[m] = pos;
        if (m % 5 == 0 && xrd_half(m / 5) >= 0) r.n_x[xrd_half(m / 5)] = endX[xrd_half(m / 5)] ? pos - endX[xrd_half(m / 5)] : -1000;
        for (int s = 0; s < 15; ++s) if (sync_m(s) == m) r.n_sync[s] = s == 14 ? 63 : endW[s + 1] ? pos - endW[s + 1] : -1000;
        if (u < nu && m >= ua[u] && m >= nextok) {
            r.kind[m] = uk[u]; r.id[m] = ui[u]; r.piece[m] = pc;
            ++pos; ++pc; nextok = m + us[u];
            int np = uk[u] == 1 ? 5 : 4;
            if (pc == np) { if (uk[u] == 1) endW[ui[u]] = pos; else endX[ui[u]] = pos; pc = 0; ++u; }
        }
        pos += xop(isa, m);
    }
    r.pos[NMG] = pos;
    r.left_stage = u < nu ? ui[u] : NST;
    return r;
}
constexpr Sched SCHED_A = make_sched(true), SCHED_B = make_sched(false);
static_assert(SCHED_A.left_stage == NST && SCHED_B.left_stage == NST, "all LDS-DMA pieces must be placed");
HD constexpr int cap63(int n) { return n > 63 ? 63 : n; }

template <int V> struct IC { static constexpr int v = V; };
template <int I, int N, class F> DI void sfor(F&& f) { if constexpr (I < N) { f(IC<I>{}); sfor<I + 1, N>(f); } }

template <bool TRANS, bool FIRST> DI void mml(f32x4& d, const u32x4& x, const u32x4& w) {
    if constexpr (FIRST) {
        if constexpr (TRANS) asm volatile("v_mfma_f32_16x16x32_bf16 %0, %2, %1, 0" : "=&v"(d) : "a"(x), "v"(w));
        else                 asm volatile("v_mfma_f32_16x16x32_bf16 %0, %1, %2, 0" : "=&v"(d) : "a"(x), "v"(w));
    } else {
        if constexpr (TRANS) asm volatile("v_mfma_f32_16x16x32_bf16 %0, %2, %1, %0" : "+v"(d) : "a"(x), "v"(w));
        else                 asm volatile("v_mfma_f32_16x16x32_bf16 %0, %1, %2, %0" : "+v"(d) : "a"(x), "v"(w));
    }
}
DI void mv0(f32x4& d, const u32x4& a, const u32x4& b) { asm volatile("v_mfma_f32_16x16x32_bf16 %0, %1, %2, 0" : "=&v"(d) : "v"(a), "v"(b)); }
DI void mva(f32x4& d, const u32x4& a, const u32x4& b) { asm volatile("v_mfma_f32_16x16x32_bf16 %0, %1, %2, %0" : "+v"(d) : "v"(a), "v"(b)); }
DI void mvc(f32x4& d, const u32x4& a, const u32x4& b, const f32x4& c) { asm volatile("v_mfma_f32_16x16x32_bf16 %0, %1, %2, %3" : "=&v"(d) : "v"(a), "v"(b), "v"(c)); }
#define PRE_MFMA()  do { SB(); asm volatile("s_nop 1"); SB(); } while (0)
#define POST_MFMA() do { SB(); asm volatile("s_nop 7"); SB(); } while (0)

DI u32 pk2(float a, float b) { f32x2 v = {a, b}; return __builtin_bit_cast(u32, __builtin_convertvector(v, bf16x2)); }
DI u32x2 pk4(f32x4 v) { u32x2 r = {pk2(v[0], v[1]), pk2(v[2], v[3])}; return r; }
DI u32x4 cat(u32x2 a, u32x2 b) { u32x4 r = {a[0], a[1], b[0], b[1]}; return r; }
DI u32x2 trread(u32 addr) {
    return __builtin_bit_cast(u32x2, __builtin_amdgcn_ds_read_tr16_b64_v4i16((LAS s16x4*)(size_t)addr)); }
template <class T> DI T lds_ld(u32 addr) { return *(LAS T*)(size_t)addr; }
template <class T> DI void lds_st(u32 addr, T v) { *(LAS T*)(size_t)addr = v; }
DI float vmax(float a, float b) { return __builtin_elementwise_maximum(a, b); }
DI float sel4(int i, float a, float b, float c, float d) { float r = a; r = i == 1 ? b : r; r = i == 2 ? c : r; r = i == 3 ? d : r; return r; }
DI u32 umax(u32 a, u32 b) { return __builtin_elementwise_max(a, b); }
DI u32 umax4(u32 m, u32x4 v) { return umax(umax(umax(umax(m, v[0]), v[1]), v[2]), v[3]); }
DI void swap16(float& a, float& b) { auto r = __builtin_amdgcn_permlane16_swap(__float_as_uint(a), __float_as_uint(b), false, false); a = __uint_as_float(r[0]); b = __uint_as_float(r[1]); }
DI void swap32(float& a, float& b) { auto r = __builtin_amdgcn_permlane32_swap(__float_as_uint(a), __float_as_uint(b), false, false); a = __uint_as_float(r[0]); b = __uint_as_float(r[1]); }
DI rsrc_t mk_rsrc(const void* p, u32 bytes) { return __builtin_amdgcn_make_buffer_rsrc((void*)p, (short)0, (int)bytes, 0x00020000); }
DI u64 mix(u64 e) { return e * 0x9E3779B97F4A7C15ull + 0x632BE59BD9B4E019ull; }

template <int OFF> DI void ld128_sc1(u32x4& d, u32 voff, rsrc_t rs, u32 soff) {
    asm volatile("buffer_load_dwordx4 %0, %1, %2, %3 offen offset:%4 sc1" : "=v"(d) : "v"(voff), "s"(rs), "s"(soff), "i"(OFF));
}
template <int OFF> DI void rld128_sc1(u32x4& d, u32 voff, rsrc_t rs, u32 soff) {
    asm volatile("buffer_load_dwordx4 %0, %1, %2, %3 offen offset:%4 sc1" : "+v"(d) : "v"(voff), "s"(rs), "s"(soff), "i"(OFF));
}
DI void ld64_sc1(u32x2& d, u32 voff, rsrc_t rs, u32 soff) {
    asm volatile("buffer_load_dwordx2 %0, %1, %2, %3 offen sc1" : "=v"(d) : "v"(voff), "s"(rs), "s"(soff));
}
template <int OFF> DI void st128_sc1(u32x4 v, u32 voff, rsrc_t rs, u32 soff) {
    asm volatile("buffer_store_dwordx4 %0, %1, %2, %3 offen offset:%4 sc1\n\ts_nop 1" :: "v"(v), "v"(voff), "s"(rs), "s"(soff), "i"(OFF) : "memory");
}
template <int OFF> DI void st64_sc1(u32x2 v, u32 voff, rsrc_t rs, u32 soff) {
    asm volatile("buffer_store_dwordx2 %0, %1, %2, %3 offen offset:%4 sc1" :: "v"(v), "v"(voff), "s"(rs), "s"(soff), "i"(OFF) : "memory");
}
template <int LDSOFF> DI void set_m0(u32 base) { asm volatile("s_add_u32 m0, %0, %1" :: "s"(base), "i"(LDSOFF) : "scc"); }
DI void glds_go(u32 voff, rsrc_t rs, u32 soff) {
    asm volatile("buffer_load_dwordx4 %0, %1, %2 offen lds" :: "v"(voff), "s"(rs), "s"(soff)); }
template <int LDSOFF> DI void glds(u32 base, u32 voff, rsrc_t rs, u32 soff) {
    asm volatile("s_add_u32 m0, %0, %4\n\ts_nop 0\n\tbuffer_load_dwordx4 %1, %2, %3 offen lds"
                 :: "s"(base), "v"(voff), "s"(rs), "s"(soff), "i"(LDSOFF) : "scc");
}
template <int N> DI void wait_vm() { asm volatile("s_waitcnt vmcnt(%0)" :: "i"(N) : "memory"); }
DI void pin(u32x4& a) { asm volatile("" : "+v"(a)); }
DI void pin(u32x2& a) { asm volatile("" : "+v"(a)); }
DI void pin(u32& a) { asm volatile("" : "+v"(a)); }
DI bool all_eq(u32x2 v, u32x2 tag) { return __builtin_amdgcn_ballot_w64((v[0] != tag[0]) | (v[1] != tag[1])) == 0; }

DI void poll(rsrc_t ws, u32 voff, u32x2 tag) {
#pragma unroll 1
    for (int it = 0; it < (1 << 18); ++it) {
        u32x2 v;
        asm volatile("buffer_load_dwordx2 %0, %1, %2, 0 offen sc1\n\ts_waitcnt vmcnt(0)" : "=v"(v) : "v"(voff), "s"(ws) : "memory");
        if (all_eq(v, tag)) break;
        __builtin_amdgcn_s_sleep(1);
    }
}

#define TS(i) do {} while (0)

DI u32 phi2(float a, float b) {
    f32x2 t = {a, b};
    f32x2 y = t * 1.4426950408889634f;
    f32x2 u = t + 1.0f;
    f32x2 r = {vmax(u[0], __builtin_amdgcn_fmed3f(__builtin_amdgcn_exp2f(y[0]), 0.f, 1.f)),
               vmax(u[1], __builtin_amdgcn_fmed3f(__builtin_amdgcn_exp2f(y[1]), 0.f, 1.f))};
    r = r + 1e-6f;
    return pk2(r[0], r[1]);
}

template <bool ISA>
DI void body(const u16* __restrict__ xg, const u16* __restrict__ wg_, const u16* __restrict__ wog, u16* __restrict__ outg,
             char* wsp, int t, int g, char* smem) {
    constexpr const Sched& SC = ISA ? SCHED_A : SCHED_B;
    const int tid = threadIdx.x;
    const int lane = tid & 63;
    const int w = __builtin_amdgcn_readfirstlane(tid >> 6);
    const int gam = lane >> 4, c16 = lane & 15, hc = c16 >> 2;
    const int b = ISA ? 0 : 1;
    const int wg = (b * 16 + t) * 8 + g;
    const u32 lds0 = (u32)(size_t)(LAS char*)smem;
    TS(0);

    u32 cpf;
    {
        u64 pc; u32 len;
        if constexpr (ISA) asm volatile("s_getpc_b64 %0\n.Lfk_pc_a:\n\ts_mov_b32 %1, .Lfk_end_a-.Lfk_pc_a" : "=s"(pc), "=s"(len));
        else               asm volatile("s_getpc_b64 %0\n.Lfk_pc_b:\n\ts_mov_b32 %1, .Lfk_end_b-.Lfk_pc_b" : "=s"(pc), "=s"(len));
        len = len > 65536u ? 0u : len;
        const rsrc_t rsC = mk_rsrc((const void*)pc, len);
        asm volatile("buffer_load_dword %0, %1, %2, 0 offen" : "=v"(cpf) : "v"(lane < 16 ? (u32)((g & 3) + 4 * (lane + 16 * w)) * 128u : OOB), "s"(rsC));
    }
    SB();
    const rsrc_t rsW = mk_rsrc(wg_, DM * 3 * DM * 2);
    const rsrc_t rsX = mk_rsrc(xg + (size_t)(t * 128 + w * 32) * DM, 32 * DM * 2);
    const int rot = (t + g) % 5;
    u32 rck[5];
#pragma unroll
    for (int j = 0; j < 5; ++j) { int c = j + rot; c = c >= 5 ? c - 5 : c; rck[j] = (u32)c; }
    const u32 ldsw = lds0 + (u32)w * 5120u;
    const u32 ldsx = lds0 + XST_OFF + (u32)w * 20480u;
    u32 vx[4];
#pragma unroll
    for (int j = 0; j < 4; ++j) { int i = 8 * (j & 1) + (lane >> 3); vx[j] = (u32)((2 * i + (j >> 1)) * 1280 + (((lane & 7) ^ (i >> 1)) * 16)); }
    auto soffX = [&](auto H) { return rck[H.v >> 1] * 256u + (u32)(H.v & 1) * 128u; };
    auto issueX = [&](auto H) { const u32 so = soffX(H); sfor<0, 4>([&](auto J) { glds<(H.v % 5) * 4096 + J.v * 1024>(ldsx, vx[J.v], rsX, so); }); };
    issueX(IC<0>{});
    SB();
    u32 vw[5];
#pragma unroll
    for (int i = 0; i < 5; ++i) {
        int c = i * 64 + lane;
        int rl = c / 10, ch = c - rl * 10;
        vw[i] = (u32)((rl ^ ((rl >> 1) & 4)) * 3840 + ch * 16);
    }
    const u32 wbase = (u32)w * (32u * 3840u) + (u32)g * 160u;
    auto soffW = [&](auto S) { return rck[st_chunk(S.v)] * (128u * 3840u) + (u32)(st_type(S.v) * 1280) + wbase; };
    auto issueW = [&](auto S) { const u32 so = soffW(S); sfor<0, 5>([&](auto I) { glds<st_slot(S.v) * SLOT_B + I.v * 1024>(ldsw, vw[I.v], rsW, so); }); };
    issueW(IC<0>{});
    const rsrc_t rsWo = mk_rsrc(wog, DM * DM * 2);
    const rsrc_t rsWS = mk_rsrc(wsp, WS_BYTES);
    u16* const outp = outg + (size_t)(t * 128 + w * 32) * (2 * DM) + b * DM + g * 80;
    const rsrc_t rsO = mk_rsrc(outp, 31 * 2 * DM * 2 + 160);
    u32x2 ep; u32 touch;
    {
        const u32 tv = lane == 0 ? (ISA ? (u32)(128 * (1 + t)) : 0u) : lane == 1 ? (ISA ? (u32)(128 * t) : 0u)
                     : lane == 2 ? WS_O + (u32)((b * 16 + t) * 4 + w) * OBLK + 40960u : lane == 3 ? WS_P1 + (u32)wg * 8192u + 8064u : lane == 4 ? WS_O - 128u : OOB;
        ld64_sc1(ep, tv, rsWS, 0u);
        const rsrc_t rsT = mk_rsrc((w & 1) ? (const void*)wog : (const void*)outp, (w & 1) ? DM * DM * 2 : 160);
        asm volatile("buffer_load_dword %0, %1, %2, 0 offen" : "=v"(touch) : "v"((u32)lane * 65536u), "s"(rsT));
    }
    SB();

    u32 vo[5], vo4[5];
#pragma unroll
    for (int i = 0; i < 5; ++i) {
        int c = i * 64 + lane;
        int rl = c / 10, ch = c - rl * 10;
        int k = rl ^ ((rl >> 1) & 4);
        int ga = k >> 3, e = k & 7;
        vo[i] = (u32)((20 * (e >> 2) + 4 * ga + (e & 3)) * 1280 + ch * 16);
        vo4[i] = (u32)((80 * (e >> 2) + 20 * ga + 16 + (e & 3)) * 1280 + ch * 16);
        asm volatile("" : "+v"(vo[i]), "+v"(vo4[i]));
    }
    auto soffO = [&](auto S) {
        constexpr int ck = st_chunk(S.v);
        const u32 row = ck < 4 ? 80u * (u32)((g + 2 * ck + (w >> 1)) & 7) + 40u * (u32)(w & 1) : 160u * (u32)(((g >> 1) + w) & 3);
        return row * 1280u + (u32)g * 160u;
    };

    u32 rb[2][2][2];
    {
        int q = (lane >> 2) & 3, p = lane & 3;
#pragma unroll
        for (int tt = 0; tt < 2; ++tt) {
            int row = 8 * gam + 4 * (tt ^ (gam & 1)) + q;
            rb[0][tt][0] = lds0 + (u32)(row * 160 + 8 * p);
            rb[0][tt][1] = lds0 + (u32)(row * 160 + 40 * p + 32);
            rb[1][tt][0] = rb[0][tt][0] + 3 * SLOT_B;
            rb[1][tt][1] = rb[0][tt][1] + 3 * SLOT_B;
            asm volatile("" : "+v"(rb[0][tt][0]), "+v"(rb[0][tt][1]), "+v"(rb[1][tt][0]), "+v"(rb[1][tt][1]));
        }
    }
    u32x2 Bh[2][5][2];
    auto readB = [&](auto N, auto F, auto T) {
        constexpr int n = N.v, s = n >> 2, kk = n & 3, slot = st_slot(s), hf = slot / 3, f = F.v;
        constexpr int off = (slot % 3) * SLOT_B + kk * 5120;
        Bh[n & 1][f][T.v] = f < 4 ? trread(rb[hf][T.v][0] + off + 40 * f) : trread(rb[hf][T.v][1] + off);
    };
    auto readBall = [&](auto N) { sfor<0, 5>([&](auto F) { readB(N, F, IC<0>{}); readB(N, F, IC<1>{}); SB(); }); };
    u32 rx[2];
#pragma unroll
    for (int kk = 0; kk < 2; ++kk) { rx[kk] = ldsx + (u32)(c16 * 128 + (((4 * kk + gam) ^ (c16 >> 1)) * 16)); asm volatile("" : "+v"(rx[kk])); }
    u32x4 xf[2][20];
    auto readX = [&](auto H, auto J) {
        constexpr int h = H.v, p = J.v & 1, kk = J.v >> 1;
        xf[p][h * 2 + kk] = lds_ld<u32x4>(rx[kk] + (h % 5) * 4096 + p * 2048);
    };

    f32x4 acc[2][5];
    const u32 xch = lds0 + XCH_OFF;
    u32 vl = (u32)lane * 16u;
    const u32 myP1 = WS_P1 + (u32)wg * 8192u;
    const u32 obase = WS_O + (u32)((b * 16 + t) * 4 + w) * OBLK;
    u32 vH = vl + (u32)(g & 1) * 8u;
    u32 vflag = lane == 0 ? 0u : OOB;
    u32 vh0 = (ISA && w == 3 && lane >= 32) ? OOB : vl;
    u32 vh1 = (ISA && w == 3) ? OOB : vl;
    u32 f1src = ISA ? WS_F1 + (u32)((t > 0 ? wg - 8 : wg) * 4 + w) * 128u : WS_F1 + (u32)((128 + g) * 4 + w) * 128u + (u32)c16 * 4096u;
    u32 f2src = WS_F2 + (u32)(((b * 16 + t) * 8 + (lane & 7)) * 4 + w) * 128u;
    u32 vst0 = xch + (u32)(w * 5120 + c16 * 320 + gam * 8), vst1 = xch + (u32)(w * 5120 + c16 * 320 + gam * 40 + 32);
    u32 vout[5];
#pragma unroll
    for (int i = 0; i < 5; ++i) { int c = i * 64 + lane; int rl = c / 10; vout[i] = (u32)(rl * 2560 + (c - rl * 10) * 16); asm volatile("" : "+v"(vout[i])); }
    u32x4 on = {0x3F803F80u, 0x3F803F80u, 0x3F803F80u, 0x3F803F80u};
    asm volatile("" : "+v"(vl), "+v"(vH), "+v"(vflag), "+v"(vh0), "+v"(vh1), "+v"(f1src), "+v"(f2src), "+v"(vst0), "+v"(vst1), "+v"(on));
    u32x2 tag, tagp;
    u32 wpf, vwpf = lane < 50 ? (u32)((((t & 3) * 8 + g) * 4 + w) * 50 + lane) * 128u : OOB;
    asm volatile("" : "+v"(vwpf));

    f32x4 cown, cprv; u32x2 mown, mprv;
    u32 kwr, vwr, krd, vrd;
    u32x4 S1, S2;
    u32 a1m[4], a2m[4], a2n[4], a3m[4], l8, zrd;
    if constexpr (ISA) {
        const bool hasprev = (w > 0) || (t > 0);
        bool vo_[4], vp_[4];
#pragma unroll
        for (int j = 0; j < 4; ++j) {
            int kk = 4 * gam + j;
            vo_[j] = (kk <= c16) && (kk >= c16 - 7);
            vp_[j] = (kk >= c16 + 9) && hasprev;
            cown[j] = vo_[j] ? 0.f : -1e30f;
            cprv[j] = vp_[j] ? 0.f : -1e30f;
        }
        mown[0] = (vo_[0] ? 0xFFFFu : 0u) | (vo_[1] ? 0xFFFF0000u : 0u);
        mown[1] = (vo_[2] ? 0xFFFFu : 0u) | (vo_[3] ? 0xFFFF0000u : 0u);
        mprv[0] = (vp_[0] ? 0xFFFFu : 0u) | (vp_[1] ? 0xFFFF0000u : 0u);
        mprv[1] = (vp_[2] ? 0xFFFFu : 0u) | (vp_[3] ? 0xFFFF0000u : 0u);
        kwr = xch + (c16 >= 8 ? (u32)(w + 1) * 8192u : 0u) + (u32)(gam * 8 + (c16 & 7)) * 16u;
        vwr = xch + (gam >= 2 ? (u32)(w + 1) * 8192u : 0u) + 4096u + (u32)(lane & 31) * 8u;
        krd = xch + (u32)w * 8192u + (u32)(gam * 8 + (c16 & 7)) * 16u;
        vrd = xch + (u32)w * 8192u + 4096u + (u32)(lane & 31) * 8u;
        asm volatile("" : "+v"(cown), "+v"(cprv), "+v"(mown), "+v"(mprv), "+v"(kwr), "+v"(vwr), "+v"(krd), "+v"(vrd));
    } else {
        const u32 one = (c16 & 1) ? 0x3F800000u : 0x00003F80u;
        const u32 a0 = (gam == hc && !(c16 & 2)) ? one : 0u, a1 = (gam == hc && (c16 & 2)) ? one : 0u;
        S1 = (u32x4){a0, a1, 0u, 0u}; S2 = (u32x4){0u, 0u, a0, a1};
        l8 = xch + 32768u + (u32)lane * 8u; zrd = xch + 32768u + (u32)(6 * 512) + (u32)gam * 128u;
        const u32 zb = xch + 32768u + 4096u;
#pragma unroll
        for (int h = 0; h < 4; ++h) {
            a1m[h] = gam == h ? l8 + 4 * 512 : zb;
            a2m[h] = hc == h ? l8 + 5 * 512 : zb;
            a2n[h] = (hc == h && gam == h) ? l8 + 7 * 512 : zb;
            a3m[h] = gam == h ? xch + 32768u + (u32)(7 * 512 + h * 128 + ((h + 1) & 3) * 32) : zb;
            asm volatile("" : "+v"(a1m[h]), "+v"(a2m[h]), "+v"(a2n[h]), "+v"(a3m[h]));
        }
        asm volatile("" : "+v"(S1), "+v"(S2), "+v"(l8), "+v"(zrd));
    }

    u32x4 Kown[2][4], Kprv[2][4], Vop[2][5];
    u32x4 PK[5];
    u32x4 hcp[2];
    u32x2 flg;
    u32x4 gl[16];

    auto filler = [&](auto M) {
        constexpr int m = M.v;
        if constexpr (m == M_WPF) asm volatile("buffer_load_dword %0, %1, %2, 0 offen" : "=v"(wpf) : "v"(vwpf), "s"(rsWo));
        if constexpr (ISA) {
            if constexpr (m == M_PUBA) {
                hcp[0] = lds_ld<u32x4>(xch + 4 * 8192 + (u32)w * 2048u + vl);
                hcp[1] = lds_ld<u32x4>(xch + 4 * 8192 + (u32)w * 2048u + vl + 1024);
            }
            if constexpr (m == M_PUBA + 4) st128_sc1<0>(hcp[0], vh0, rsWS, myP1 + (u32)w * 2048u);
            if constexpr (m == M_PUBA + 5) st128_sc1<1024>(hcp[1], vh1, rsWS, myP1 + (u32)w * 2048u);
            if constexpr (m >= 295) {
                constexpr int i = m - 295;
                if constexpr (i < 4) { Kprv[i >> 1][(i & 1) * 2] = lds_ld<u32x4>(krd + 2 * i * 512); Kprv[i >> 1][(i & 1) * 2 + 1] = lds_ld<u32x4>(krd + (2 * i + 1) * 512); }
#pragma unroll
                for (int q = 2 * i; q < 2 * i + 2; ++q) { u32x2 v = lds_ld<u32x2>(vrd + q * 256); Vop[q / 5][q % 5][2] = v[0]; Vop[q / 5][q % 5][3] = v[1]; }
            }
        } else {
            if constexpr (m >= M_CHK && m < M_CHK + 16) ld128_sc1<0>(gl[m - M_CHK], vl, rsWS, WS_P1 + (u32)((16 + ((m - M_CHK + 4 * t) & 15)) * 8 + g) * 8192u + (u32)w * 1024u);
        }
        if constexpr (m == M_POLL) ld64_sc1(flg, f1src, rsWS, 0u);
    };

    auto sync = [&](auto S, auto LG) {
        constexpr int s = S.v;
        static_assert(SC.n_sync[s] >= 0, "W piece not issued yet");
        constexpr int xc = sync_m(s) % 20 == 15 ? xrd_half(sync_m(s) / 5) : -1;
        constexpr int nw = (xc >= 0 && SC.n_x[xc < 0 ? 0 : xc] < SC.n_sync[s]) ? SC.n_x[xc < 0 ? 0 : xc] : SC.n_sync[s];
        SB();
        if constexpr (LG.v) asm volatile("s_waitcnt vmcnt(%0) lgkmcnt(0)\n\ts_barrier" :: "i"(cap63(nw)) : "memory");
        else asm volatile("s_waitcnt vmcnt(%0)\n\ts_barrier" :: "i"(cap63(nw)) : "memory");
        SB();
    };
    auto group = [&](auto M) {
        constexpr int m = M.v, n = m / 5, f = m % 5, s = n >> 2, kk = n & 3, ty = st_type(s), ck = st_chunk(s);
        constexpr bool trans = ty == 0 || (ISA && ty == 1);
        constexpr bool first = ck == 0 && kk == 0;
        constexpr bool pre = (n + 1) % 20 != 0;
        constexpr int xc = xrd_half(n);
        static_assert(xc < 0 || SC.n_x[xc < 0 ? 0 : xc] >= 0, "x piece not issued yet");
        if constexpr (m == sync_m(s)) sync(IC<s>{}, IC<(ISA && s == 14)>{});
        else if constexpr (f == 0 && xc >= 0) { wait_vm<cap63(SC.n_x[xc < 0 ? 0 : xc])>(); SB(); }
        constexpr int vk = SC.kind[m], vi = SC.id[m], vp = SC.piece[m];
        const u32x4 Bw = cat(Bh[n & 1][f][0], Bh[n & 1][f][1]);
        if constexpr (vk == 1) set_m0<st_slot(vi) * SLOT_B + vp * 1024>(ldsw);
        if constexpr (vk == 2) set_m0<(vi % 5) * 4096 + vp * 1024>(ldsx);
        mml<trans, first>(acc[0][f], xf[0][ck * 4 + kk], Bw);
        SB();
        if constexpr (pre) readB(IC<n + 1>{}, IC<f>{}, IC<0>{});
        if constexpr (vk == 1) {
            if constexpr (st_type(vi) == 3) glds_go(st_chunk(vi) < 4 ? vo[vp] : vo4[vp], rsWo, soffO(IC<vi>{}));
            else glds_go(vw[vp], rsW, soffW(IC<vi>{}));
        }
        if constexpr (vk == 2) glds_go(vx[vp], rsX, soffX(IC<vi>{}));
        if constexpr (xc >= 0 && f < 4) readX(IC<xc>{}, IC<f>{});
        filler(M);
        SB();
        mml<trans, first>(acc[1][f], xf[1][ck * 4 + kk], Bw);
        SB();
        if constexpr (pre) readB(IC<n + 1>{}, IC<f>{}, IC<1>{});
        SB();
    };

    SB();
    issueX(IC<1>{}); issueW(IC<1>{}); issueX(IC<2>{});
    SB();
    wait_vm<SC.n_init>();
    sfor<0, 4>([&](auto J) { readX(IC<0>{}, J); });
    asm volatile("s_barrier" ::: "memory");
    TS(1);
    readBall(IC<0>{});

    sfor<0, 100>(group);
    POST_MFMA();
    {
        pin(ep); pin(touch); pin(cpf);
        u64 a = mix(((u64)(u32)__builtin_amdgcn_readlane(ep[1], 0) << 32) | (u32)__builtin_amdgcn_readlane(ep[0], 0));
        u64 c = mix(((u64)(u32)__builtin_amdgcn_readlane(ep[1], 1) << 32) | (u32)__builtin_amdgcn_readlane(ep[0], 1));
        tag = (u32x2){(u32)a, (u32)(a >> 32)}; tagp = (u32x2){(u32)c, (u32)(c >> 32)};
        pin(tag);
    }
    if constexpr (ISA) {
#pragma unroll
        for (int p = 0; p < 2; ++p)
#pragma unroll
            for (int h = 0; h < 4; ++h) {
                f32x4 k4 = acc[p][4]; asm volatile("" : "+v"(k4));
                Kown[p][h] = cat(pk4(acc[p][h]), pk4(k4)); pin(Kown[p][h]);
            }
    } else {
#pragma unroll
        for (int f = 0; f < 5; ++f) {
            PK[f] = (u32x4){phi2(acc[0][f][0], acc[0][f][1]), phi2(acc[0][f][2], acc[0][f][3]), phi2(acc[1][f][0], acc[1][f][1]), phi2(acc[1][f][2], acc[1][f][3])};
            pin(PK[f]);
        }
    }
    sync(IC<4>{}, IC<0>{});
    readBall(IC<20>{});
    if constexpr (ISA) {
#pragma unroll
        for (int i = 0; i < 8; ++i) lds_st(kwr + i * 512, Kown[i >> 2][i & 3]);
        SB();
    }
    sfor<100, 200>(group);
    POST_MFMA();
    TS(2);
    if constexpr (ISA) {
#pragma unroll
        for (int i = 0; i < 10; ++i) {
            u32x2 v = pk4(acc[i / 5][i % 5]);
            Vop[i / 5][i % 5][0] = v[0]; Vop[i / 5][i % 5][1] = v[1];
            lds_st(vwr + i * 256, v);
        }
        sync(IC<9>{}, IC<1>{});
        readBall(IC<40>{});
    } else {
        u32x4 VV[5];
        f32x4 G[8], d1[4], d2[4], zz[4], d3, z4;
        VV[4] = cat(pk4(acc[0][4]), pk4(acc[1][4]));
        PRE_MFMA();
        sfor<0, 4>([&](auto H) {
            constexpr int h = H.v;
            mv0(zz[h], PK[h], on);
            SB();
            VV[h][0] = pk2(acc[0][h][0], acc[0][h][1]); VV[h][1] = pk2(acc[0][h][2], acc[0][h][3]);
            SB();
            mv0(d2[h], PK[h], VV[4]);
            SB();
            VV[h][2] = pk2(acc[1][h][0], acc[1][h][1]); VV[h][3] = pk2(acc[1][h][2], acc[1][h][3]);
            SB();
        });
        mv0(d3, PK[4], VV[4]);
        mv0(z4, PK[4], on);
        SB();
        sfor<0, 4>([&](auto H) {
            constexpr int h = H.v;
            mv0(d1[h], PK[4], VV[h]);
            SB();
            G[6][h] = sel4(hc, zz[0][h], zz[1][h], zz[2][h], zz[3][h]);
            SB();
            mv0(G[h], PK[h], VV[h]);
            SB();
            G[5][h] = sel4(hc, d2[0][h], d2[1][h], d2[2][h], d2[3][h]);
            SB();
        });
#pragma unroll
        for (int j = 0; j < 4; ++j) G[7][j] = gam == hc ? d3[j] : z4[j];
        const u32 wr = xch + (u32)w * 8192u + vl;
        lds_st(wr + 5 * 1024, G[5]); lds_st(wr + 6 * 1024, G[6]); lds_st(wr + 7 * 1024, G[7]);
        lds_st(l8 + 4096u, (u32x2){0u, 0u});
        POST_MFMA();
#pragma unroll
        for (int i = 0; i < 4; ++i) lds_st(wr + i * 1024, G[i]);
#pragma unroll
        for (int j = 0; j < 4; ++j) G[4][j] = sel4(gam, d1[0][j], d1[1][j], d1[2][j], d1[3][j]);
        lds_st(wr + 4 * 1024, G[4]);
        SB();
        asm volatile("s_waitcnt vmcnt(%0) lgkmcnt(0)\n\ts_barrier" :: "i"(cap63(SC.n_sync[9] - 1)) : "memory");
        SB();
        f32x4 s[2];
#pragma unroll
        for (int i = 0; i < 2; ++i) {
            const u32 rd = xch + (u32)w * 2048u + vl + i * 1024;
            s[i] = (lds_ld<f32x4>(rd) + lds_ld<f32x4>(rd + 8192)) + (lds_ld<f32x4>(rd + 16384) + lds_ld<f32x4>(rd + 24576));
        }
        sfor<0, 5>([&](auto F) { readB(IC<40>{}, F, IC<0>{}); readB(IC<40>{}, F, IC<1>{}); });
        st128_sc1<0>(cat(pk4(s[0]), pk4(s[1])), vl, rsWS, myP1 + (u32)w * 1024u);
        SB();
    }
    TS(3);

    sfor<200, M_FLAG>(group);
    wait_vm<SC.pos[M_FLAG] - 1 - SC.pos[ISA ? M_PUBA + 6 : 200]>();
    st64_sc1<0>(tag, vflag, rsWS, WS_F1 + (u32)(wg * 4 + w) * 128u);
    SB();
    sfor<M_FLAG, M_CHK>(group);
    TS(4);
    {
        wait_vm<SC.pos[M_CHK] - xpre(ISA, M_CHK) - SC.pos[M_POLL + 1]>();
        pin(flg); pin(wpf);
        if constexpr (ISA) {
            if (t > 0 && !all_eq(flg, tagp)) poll(rsWS, f1src, tagp);
            SB();
            const u32 so = t > 0 ? myP1 - 8u * 8192u + (u32)w * 2048u : OOB;
            ld128_sc1<0>(hcp[0], vh0, rsWS, so);
            ld128_sc1<1024>(hcp[1], vh1, rsWS, so);
        } else {
            if (!all_eq(flg, tag)) poll(rsWS, f1src, tag);
        }
        SB();
    }
    TS(5);
    sfor<M_CHK, M_PARK>(group);
    if constexpr (ISA) {
        wait_vm<SC.pos[M_PARK] - SC.pos[M_CHK]>();
        pin(hcp[0]); pin(hcp[1]);
        lds_st(xch + (u32)w * 2048u + vl, hcp[0]);
        lds_st(xch + (u32)w * 2048u + vl + 1024, hcp[1]);
        SB();
    }
    sfor<M_PARK, 300>(group);
    POST_MFMA();
    TS(6);

    u32x4 of[2][20];
    auto publish = [&](auto P, u32x2 h4) {
        constexpr int p = P.v;
        st128_sc1<0>(of[p][0], vl, rsWS, obase + (u32)p * 20480u + (u32)g * 2048u);
        st128_sc1<1024>(of[p][1], vl, rsWS, obase + (u32)p * 20480u + (u32)g * 2048u);
        st64_sc1<0>(h4, vH, rsWS, obase + (u32)p * 20480u + 16384u + (u32)(g >> 1) * 1024u);
    };
    if constexpr (ISA) {
        u32x4 Qf[2][4];
        f32x4 so[2][4], sp[2][4];
        sfor<0, 2>([&](auto P) {
            constexpr int p = P.v;
            u32x2 q4 = pk4(acc[p][4]);
            sfor<0, 4>([&](auto H) {
                constexpr int h = H.v, i = p * 4 + h;
                Qf[p][h] = cat(pk4(acc[p][h]), (u32x2){gam == h ? q4[0] : 0u, gam == h ? q4[1] : 0u});
                SB();
                if constexpr (i > 0) {
                    mvc(so[(i - 1) >> 2][(i - 1) & 3], Kown[(i - 1) >> 2][(i - 1) & 3], Qf[(i - 1) >> 2][(i - 1) & 3], cown);
                    mvc(sp[(i - 1) >> 2][(i - 1) & 3], Kprv[(i - 1) >> 2][(i - 1) & 3], Qf[(i - 1) >> 2][(i - 1) & 3], cprv);
                    SB();
                }
            });
        });
        mvc(so[1][3], Kown[1][3], Qf[1][3], cown);
        mvc(sp[1][3], Kprv[1][3], Qf[1][3], cprv);
        SB();
        asm volatile("s_nop 2" :: "v"(cown), "v"(cprv));
        SB();
        const float cs = 0.22360679774997896f * 1.4426950408889634f;
        f32x4 oh[2][4], o4[2][4], dn[2][4];
        u32x4 Pf[2][4];
        auto pv = [&](auto P, auto I) {
            constexpr int p = P.v, h = I.v / 3, k = I.v % 3;
            SB();
            if constexpr (k == 0) mv0(dn[p][h], on, Pf[p][h]); else if constexpr (k == 1) mv0(oh[p][h], Vop[p][h], Pf[p][h]); else mv0(o4[p][h], Vop[p][4], Pf[p][h]);
            SB();
        };
        sfor<0, 2>([&](auto P) {
            constexpr int p = P.v;
            f32x4 L[4]; float mx[4];
            sfor<0, 4>([&](auto H) {
                constexpr int h = H.v;
#pragma unroll
                for (int j = 0; j < 4; ++j) L[h][j] = vmax(so[p][h][j], sp[p][h][j]);
                mx[h] = vmax(vmax(vmax(L[h][0], L[h][1]), L[h][2]), L[h][3]);
                if constexpr (p == 1) pv(IC<0>{}, IC<h>{});
            });
            swap16(mx[0], mx[1]); swap16(mx[2], mx[3]);
            float xa = vmax(mx[0], mx[1]), xb = vmax(mx[2], mx[3]);
            if constexpr (p == 1) pv(IC<0>{}, IC<4>{});
            swap32(xa, xb);
            float z0 = vmax(xa, xb) * -cs, z1 = z0;
            if constexpr (p == 1) pv(IC<0>{}, IC<5>{});
            swap32(z0, z1);
            float m0 = z0, m1 = z0, m2 = z1, m3 = z1;
            if constexpr (p == 1) pv(IC<0>{}, IC<6>{});
            swap16(m0, m1); swap16(m2, m3);
            const float mc[4] = {m0, m1, m2, m3};
            if constexpr (p == 1) pv(IC<0>{}, IC<7>{});
            sfor<0, 4>([&](auto H) {
                constexpr int h = H.v;
                u32 p0 = pk2(__builtin_amdgcn_exp2f(__builtin_fmaf(L[h][0], cs, mc[h])), __builtin_amdgcn_exp2f(__builtin_fmaf(L[h][1], cs, mc[h])));
                u32 p1 = pk2(__builtin_amdgcn_exp2f(__builtin_fmaf(L[h][2], cs, mc[h])), __builtin_amdgcn_exp2f(__builtin_fmaf(L[h][3], cs, mc[h])));
                Pf[p][h] = (u32x4){p0 & mown[0], p1 & mown[1], p0 & mprv[0], p1 & mprv[1]};
                if constexpr (p == 1) pv(IC<0>{}, IC<8 + h>{});
            });
            SB();
        });
        sfor<0, 2>([&](auto P) {
            constexpr int p = P.v;
            if constexpr (p == 1) POST_MFMA();
            float r[4]; u32x2 o2[4];
            sfor<0, 4>([&](auto H) {
                constexpr int h = H.v;
                if constexpr (p == 0) { pv(IC<1>{}, IC<2 * h>{}); }
                asm volatile("" : "+v"(dn[p][h]));
                r[h] = __builtin_amdgcn_rcpf(dn[p][h][0]);
                f32x4 t4 = {oh[p][h][0] * r[h], oh[p][h][1] * r[h], oh[p][h][2] * r[h], oh[p][h][3] * r[h]};
                if constexpr (p == 0) { pv(IC<1>{}, IC<2 * h + 1>{}); }
                o2[h] = pk4(t4);
            });
            f32x4 s4;
            sfor<0, 4>([&](auto J) {
                if constexpr (p == 0) { pv(IC<1>{}, IC<8 + J.v>{}); }
                s4[J.v] = sel4(gam, o4[p][0][J.v], o4[p][1][J.v], o4[p][2][J.v], o4[p][3][J.v]);
            });
            float r4 = sel4(gam, r[0], r[1], r[2], r[3]);
            of[p][0] = cat(o2[0], o2[1]); of[p][1] = cat(o2[2], o2[3]);
            publish(P, pk4(s4 * r4));
            SB();
        });
    } else {
        u32x4 Qf[2][4];
        f32x4 s1, s2;
        u32x2 q4[2];
        sfor<0, 10>([&](auto I) {
            constexpr int i = I.v, p = i / 5, f = (i % 5 + 4) % 5;
            if constexpr (i < 8) {
                wait_vm<cap63(SC.pos[NMG] - SC.pos[M_CHK + 2 * i + 2])>(); pin(gl[2 * i]); pin(gl[2 * i + 1]); SB();
                if constexpr (i == 0) { mv0(s1, S1, gl[0]); mv0(s2, S2, gl[0]); } else { mva(s1, S1, gl[2 * i]); mva(s2, S2, gl[2 * i]); }
                SB();
            }
            if constexpr (i == 9) {
                lds_st(l8 + (u32)w * 1024u, pk4(s1));
                lds_st(l8 + (u32)w * 1024u + 512u, pk4(s2));
                SB();
            }
            u32x2 v_ = {phi2(acc[p][f][0], acc[p][f][1]), phi2(acc[p][f][2], acc[p][f][3])}; pin(v_);
            if constexpr (f == 4) q4[p] = v_; else { Qf[p][f] = cat(v_, q4[p]); pin(Qf[p][f]); }
            SB();
            if constexpr (i < 8) { mva(s1, S1, gl[2 * i + 1]); mva(s2, S2, gl[2 * i + 1]); SB(); }
        });
        asm volatile("s_waitcnt lgkmcnt(0)\n\ts_barrier" ::: "memory");
        u32x4 A1[4], A2[4], A3[4];
#pragma unroll
        for (int h = 0; h < 4; ++h) {
            A3[h] = cat(lds_ld<u32x2>(zrd + h * 32), lds_ld<u32x2>(a3m[h]));
            A1[h] = cat(lds_ld<u32x2>(l8 + h * 512), lds_ld<u32x2>(a1m[h]));
            A2[h] = cat(lds_ld<u32x2>(a2m[h]), lds_ld<u32x2>(a2n[h]));
            SB();
        }
        f32x4 nh[2][4], n4[2], dn[2][4];
        auto nd = [&](auto P, auto I) {
            constexpr int p = P.v, h = I.v / 3, k = I.v % 3;
            SB();
            if constexpr (k == 0) mv0(dn[p][h], A3[h], Qf[p][h]); else if constexpr (k == 1) mv0(nh[p][h], A1[h], Qf[p][h]);
            else if constexpr (h == 0) mv0(n4[p], A2[0], Qf[p][0]); else mva(n4[p], A2[h], Qf[p][h]);
            SB();
        };
        sfor<0, 12>([&](auto I) { nd(IC<0>{}, I); });
        sfor<0, 2>([&](auto P) {
            constexpr int p = P.v;
            if constexpr (p == 1) POST_MFMA();
            float r[4]; u32x2 o2[4];
            sfor<0, 4>([&](auto H) {
                constexpr int h = H.v;
                if constexpr (p == 0) { nd(IC<1>{}, IC<3 * h>{}); }
                asm volatile("" : "+v"(dn[p][h]));
                r[h] = __builtin_amdgcn_rcpf(dn[p][h][0] + 1e-6f);
                if constexpr (p == 0) { nd(IC<1>{}, IC<3 * h + 1>{}); }
                f32x4 t4 = {nh[p][h][0] * r[h], nh[p][h][1] * r[h], nh[p][h][2] * r[h], nh[p][h][3] * r[h]};
                if constexpr (p == 0) { nd(IC<1>{}, IC<3 * h + 2>{}); }
                o2[h] = pk4(t4);
            });
            float r4 = sel4(gam, r[0], r[1], r[2], r[3]);
            of[p][0] = cat(o2[0], o2[1]); of[p][1] = cat(o2[2], o2[3]);
            publish(P, pk4(n4[p] * r4));
            SB();
        });
    }
    TS(7);
    sfor<15 + NOIN, 20>([&](auto S) {
        const u32 so = soffO(S);
        sfor<0, 5>([&](auto I) { glds<st_slot(S.v) * SLOT_B + I.v * 1024>(ldsw, st_chunk(S.v) < 4 ? vo[I.v] : vo4[I.v], rsWo, so); });
    });
    wait_vm<5 * (5 - NOIN)>();
    st64_sc1<0>(tag, vflag, rsWS, WS_F2 + (u32)(wg * 4 + w) * 128u);
    wait_vm<1>();
    asm volatile("s_barrier" ::: "memory");
    TS(8);
#define OAHEAD 5
    auto oload = [&](auto K, auto P) {
        constexpr int k = K.v;
        const u32 so = obase + (u32)P.v * 20480u + (k < 16 ? (u32)((g + k / 2) & 7) * 2048u : 16384u + (u32)(((g >> 1) + k - 16) & 3) * 1024u);
        ld128_sc1<(k < 16 ? (k & 1) * 1024 : 0)>(of[P.v][k], vl, rsWS, so);
    };
    auto kstep = [&](auto K) {
        constexpr int k = K.v, n = 60 + k;
        sfor<0, 5>([&](auto F) {
            constexpr int f = F.v;
            const u32x4 Bw = cat(Bh[n & 1][f][0], Bh[n & 1][f][1]);
            if constexpr (k == 0) mv0(acc[0][f], Bw, of[0][k]); else mva(acc[0][f], Bw, of[0][k]);
            SB();
            if constexpr (k < 19) readB(IC<n + 1>{}, F, IC<0>{});
            if constexpr (k >= 2 && k + OAHEAD < 20 && (f == 1 || f == 3)) oload(IC<k + OAHEAD>{}, IC<(f >> 1)>{});
            SB();
            if constexpr (k == 0) mv0(acc[1][f], Bw, of[1][k]); else mva(acc[1][f], Bw, of[1][k]);
            SB();
            if constexpr (k < 19) readB(IC<n + 1>{}, F, IC<1>{});
            SB();
        });
    };
    readBall(IC<60>{});
    sfor<0, 2>(kstep);
    poll(rsWS, f2src, tag);
    TS(9);
    sfor<2, 2 + OAHEAD>([&](auto K) { oload(K, IC<0>{}); oload(K, IC<1>{}); });
    SB();
    sfor<2, 20>([&](auto K) {
        constexpr int last = K.v + OAHEAD - 1 < 19 ? K.v + OAHEAD - 1 : 19;
        wait_vm<2 * (last - K.v)>(); pin(of[0][K.v]); pin(of[1][K.v]); SB();
        kstep(K);
    });
    POST_MFMA();
    TS(10);
#pragma unroll
    for (int p = 0; p < 2; ++p) {
#pragma unroll
        for (int f = 0; f < 4; ++f) lds_st(vst0 + (p * 160 + f * 40), pk4(acc[p][f]));
        lds_st(vst1 + p * 160, pk4(acc[p][4]));
    }
#pragma unroll
    for (int i = 0; i < 5; ++i)
        __builtin_amdgcn_raw_buffer_store_b128(lds_ld<u32x4>(xch + (u32)w * 5120u + vl + i * 1024), rsO, vout[i], 0, OAUX);
    if (g == 0 && w == 0) {
        if constexpr (ISA) {
            if (t > 0) st64_sc1<0>(tagp, vflag, rsWS, WS_EPOCH + (u32)(128 * t));
            if (t == 15) st64_sc1<0>(tag, vflag, rsWS, WS_EPOCH + (u32)(128 * 16));
        } else {
            if (t == 0) st64_sc1<0>(tag, vflag, rsWS, WS_EPOCH);
        }
    }
    TS(11);
    if constexpr (ISA) asm volatile(".Lfk_end_a:"); else asm volatile(".Lfk_end_b:");
}

__global__ __launch_bounds__(256, 1) void fused_kernel(const u16* __restrict__ xa, const u16* __restrict__ wa, const u16* __restrict__ woa,
                                                       const u16* __restrict__ xb, const u16* __restrict__ wb, const u16* __restrict__ wob,
                                                       u16* __restrict__ out, char* ws) {
    __shared__ __attribute__((aligned(16))) char smem[LDS_B];
    const int x = blockIdx.x & 7, idx = blockIdx.x >> 3;
    const int t = (x & 3) * 4 + (idx >> 3), g = idx & 7;
    const bool isa = x < 4;
    const u16* xp = isa ? xa : xb; const u16* wp = isa ? wa : wb; const u16* wop = isa ? woa : wob;
    asm volatile("" : "+s"(xp), "+s"(wp), "+s"(wop), "+s"(out), "+s"(ws));
    if (isa) body<true>(xp, wp, wop, out, ws, t, g, smem);
    else     body<false>(xp, wp, wop, out, ws, t, g, smem);
}

extern "C" void kernel_launch(void* const* d_in, const int* in_sizes, int n_in,
                              void* d_out, int out_size, void* d_ws, size_t ws_size,
                              hipStream_t stream) {
    fused_kernel<<<256, 256, 0, stream>>>((const u16*)d_in[0], (const u16*)d_in[1], (const u16*)d_in[2],
                                          (const u16*)d_in[3], (const u16*)d_in[4], (const u16*)d_in[5],
                                          (u16*)d_out, (char*)d_ws);
}
```

```cpp
#include <hip/hip_runtime.h>
#include <stdint.h>

typedef unsigned short u16;
typedef unsigned int u32;
typedef unsigned long long u64;
typedef __attribute__((ext_vector_type(4))) short s16x4;
typedef __attribute__((ext_vector_type(2))) __bf16 bf16x2;
typedef __attribute__((ext_vector_type(4))) float f32x4;
typedef __attribute__((ext_vector_type(2))) float f32x2;
typedef __attribute__((ext_vector_type(4))) u32 u32x4;
typedef __attribute__((ext_vector_type(2))) u32 u32x2;
typedef __amdgpu_buffer_rsrc_t rsrc_t;
#define LAS __attribute__((address_space(3)))
#define GAS __attribute__((address_space(1)))
#define DI __device__ __forceinline__
#define HD __host__ __device__
#define SB() __builtin_amdgcn_sched_barrier(0)

#define OAUX 2
#define XMAP 2

constexpr int DM = 640;
constexpr int SLOT_B = 20480;
constexpr int XST_OFF = 4 * SLOT_B;
constexpr int XCH_OFF = 6 * SLOT_B;
constexpr int LDS_B = 163840;
constexpr u32 OOB = 0x7FFFFF00u;

constexpr u32 WS_EPOCH = 0;
constexpr u32 WS_F1 = 4096;
constexpr u32 WS_F2 = WS_F1 + 256 * 4 * 128;
constexpr u32 WS_P1 = WS_F2 + 256 * 4 * 128;
constexpr u32 WS_O = WS_P1 + 256 * 8192;
constexpr u32 OBLK = 2 * 20480 + 128;
constexpr u32 WS_TS = WS_O + 2 * 16 * 4 * OBLK;
constexpr u32 WS_TS2 = WS_TS + 256 * 4 * 16 * 8;
constexpr u32 WS_BYTES = WS_TS2 + 256 * 4 * 64 * 4;

constexpr int NST = 20;
HD constexpr int st_type(int s)  { return s < 5 ? 1 : s < 10 ? 2 : s < 15 ? 0 : 3; }
HD constexpr int st_chunk(int s) { return s % 5; }
HD constexpr int st_slot(int s)  { return s < 8 ? s % 4 : (s - 4) % 6; }
HD constexpr int sync_m(int s) { return (s == 4 || s == 9) ? 20 * s + 20 : 20 * s + 15; }
HD constexpr int xrd_half(int n) { return (n & 1) && n < 19 ? (n + 1) / 2 : -1; }

constexpr int NMG = 300;
#ifndef M_WPF
#define M_WPF 141
#endif
constexpr int M_PUBA = 200;
#ifndef M_FLAG
#define M_FLAG 222
#define M_POLL 240
#define M_CHK 260
#endif
constexpr int M_PARK = 293;
#define LOOKA 50
#define OSPACE 4
#define SPV 2
#define SPQ0 2
#ifndef M_Q1
#define M_Q1 180
#endif
#define SPQ 3
#define NOIN 3
struct Sched {
    int kind[NMG], id[NMG], piece[NMG];
    int pos[NMG + 1];
    int n_init, n_x[10], n_sync[NST];
    int left_stage;
};
HD constexpr int xpre(bool isa, int m) { return (!isa && m == 200) ? 1 : m == M_FLAG ? 1 : (isa && m == M_CHK) ? 2 : 0; }
HD constexpr int xop(bool isa, int m) {
    if (m == M_WPF) return 1;
    if (!isa && m >= M_CHK && m < M_CHK + 16) return 1;
    if (isa && m >= M_PUBA + 4 && m < M_PUBA + 6) return 1;
    if (m == M_POLL || m == M_POLL - 1) return 1;
    return 0;
}
HD constexpr Sched make_sched(bool isa) {
    Sched r{};
    int uk[32] = {}, ui[32] = {}, ua[32] = {}, us[32] = {}; int nu = 0;
    auto add = [&](int k, int i, int a, int sp) { uk[nu] = k; ui[nu] = i; ua[nu] = a; us[nu] = sp; ++nu; };
    auto mx = [](int x, int y) { return x > y ? x : y; };
    auto xav = [&](int h) { return mx(h < 5 ? 0 : h == 5 ? 5 : 5 * (2 * (h - 5) + 1), 5 * (2 * h - 1) - LOOKA); };
    auto wfree = [](int q) { return q < 4 ? 0 : q < 8 ? sync_m(q - 3) : q < 10 ? sync_m(4) : sync_m(q - 5); };
    auto wav = [&](int q) { return mx(wfree(q), sync_m(q - 1) - LOOKA); };
    add(2, 3, xav(3), 1);
    for (int q = 2; q < 5; ++q) { add(1, q, wav(q), 1); add(2, 2 * q, xav(2 * q), 1); add(2, 2 * q + 1, xav(2 * q + 1), 1); }
    add(1, 5, wav(5), 1); add(1, 6, wav(6), 1);
    for (int q = 7; q < 10; ++q) add(1, q, wfree(q), SPV);
    add(1, 10, wfree(10), SPQ0); add(1, 11, mx(wfree(11), M_Q1), SPQ);
    for (int q = 12; q < 15; ++q) add(1, q, wfree(q), SPQ);
    for (int q = 15; q < 15 + NOIN; ++q) add(1, q, wfree(q), OSPACE);
    int endW[NST] = {}, endX[10] = {};
    int pos = 0;
    pos += 4; endX[0] = pos; pos += 5; endW[0] = pos; pos += 2; pos += 4; endX[1] = pos;
    pos += 5; endW[1] = pos; pos += 4; endX[2] = pos;
    r.n_init = pos - endW[0];
    int u = 0, pc = 0, nextok = 0;
    for (int m = 0; m < NMG; ++m) {
        pos += xpre(isa, m);
        r.pos[m] = pos;
        if (m % 5 == 0 && xrd_half(m / 5) >= 0) r.n_x[xrd_half(m / 5)] = endX[xrd_half(m / 5)] ? pos - endX[xrd_half(m / 5)] : -1000;
        for (int s = 0; s < 15; ++s) if (sync_m(s) == m) r.n_sync[s] = s == 14 ? 63 : endW[s + 1] ? pos - endW[s + 1] : -1000;
        if (u < nu && m >= ua[u] && m >= nextok) {
            r.kind[m] = uk[u]; r.id[m] = ui[u]; r.piece[m] = pc;
            ++pos; ++pc; nextok = m + us[u];
            int np = uk[u] == 1 ? 5 : 4;
            if (pc == np) { if (uk[u] == 1) endW[ui[u]] = pos; else endX[ui[u]] = pos; pc = 0; ++u; }
        }
        pos += xop(isa, m);
    }
    r.pos[NMG] = pos;
    r.left_stage = u < nu ? ui[u] : NST;
    return r;
}
constexpr Sched SCHED_A = make_sched(true), SCHED_B = make_sched(false);
static_assert(SCHED_A.left_stage == NST && SCHED_B.left_stage == NST, "all LDS-DMA pieces must be placed");
HD constexpr int cap63(int n) { return n > 63 ? 63 : n; }

template <int V> struct IC { static constexpr int v = V; };
template <int I, int N, class F> DI void sfor(F&& f) { if constexpr (I < N) { f(IC<I>{}); sfor<I + 1, N>(f); } }

template <bool TRANS, bool FIRST> DI void mml(f32x4& d, const u32x4& x, const u32x4& w) {
    if constexpr (FIRST) {
        if constexpr (TRANS) asm volatile("v_mfma_f32_16x16x32_bf16 %0, %2, %1, 0" : "=&v"(d) : "a"(x), "v"(w));
        else                 asm volatile("v_mfma_f32_16x16x32_bf16 %0, %1, %2, 0" : "=&v"(d) : "a"(x), "v"(w));
    } else {
        if constexpr (TRANS) asm volatile("v_mfma_f32_16x16x32_bf16 %0, %2, %1, %0" : "+v"(d) : "a"(x), "v"(w));
        else                 asm volatile("v_mfma_f32_16x16x32_bf16 %0, %1, %2, %0" : "+v"(d) : "a"(x), "v"(w));
    }
}
DI void mv0(f32x4& d, const u32x4& a, const u32x4& b) { asm volatile("v_mfma_f32_16x16x32_bf16 %0, %1, %2, 0" : "=&v"(d) : "v"(a), "v"(b)); }
DI void mva(f32x4& d, const u32x4& a, const u32x4& b) { asm volatile("v_mfma_f32_16x16x32_bf16 %0, %1, %2, %0" : "+v"(d) : "v"(a), "v"(b)); }
DI void mvc(f32x4& d, const u32x4& a, const u32x4& b, const f32x4& c) { asm volatile("v_mfma_f32_16x16x32_bf16 %0, %1, %2, %3" : "=&v"(d) : "v"(a), "v"(b), "v"(c)); }
#define PRE_MFMA()  do { SB(); asm volatile("s_nop 1"); SB(); } while (0)
#define POST_MFMA() do { SB(); asm volatile("s_nop 7"); SB(); } while (0)

DI u32 pk2(float a, float b) { f32x2 v = {a, b}; return __builtin_bit_cast(u32, __builtin_convertvector(v, bf16x2)); }
DI u32x2 pk4(f32x4 v) { u32x2 r = {pk2(v[0], v[1]), pk2(v[2], v[3])}; return r; }
DI u32x4 cat(u32x2 a, u32x2 b) { u32x4 r = {a[0], a[1], b[0], b[1]}; return r; }
DI u32x2 trread(u32 addr) {
    return __builtin_bit_cast(u32x2, __builtin_amdgcn_ds_read_tr16_b64_v4i16((LAS s16x4*)(size_t)addr)); }
template <class T> DI T lds_ld(u32 addr) { return *(LAS T*)(size_t)addr; }
template <class T> DI void lds_st(u32 addr, T v) { *(LAS T*)(size_t)addr = v; }
DI float vmax(float a, float b) { return __builtin_elementwise_maximum(a, b); }
DI float sel4(int i, float a, float b, float c, float d) { float r = a; r = i == 1 ? b : r; r = i == 2 ? c : r; r = i == 3 ? d : r; return r; }
DI u32 umax(u32 a, u32 b) { return __builtin_elementwise_max(a, b); }
DI u32 umax4(u32 m, u32x4 v) { return umax(umax(umax(umax(m, v[0]), v[1]), v[2]), v[3]); }
DI void swap16(float& a, float& b) { auto r = __builtin_amdgcn_permlane16_swap(__float_as_uint(a), __float_as_uint(b), false, false); a = __uint_as_float(r[0]); b = __uint_as_float(r[1]); }
DI void swap32(float& a, float& b) { auto r = __builtin_amdgcn_permlane32_swap(__float_as_uint(a), __float_as_uint(b), false, false); a = __uint_as_float(r[0]); b = __uint_as_float(r[1]); }
DI rsrc_t mk_rsrc(const void* p, u32 bytes) { return __builtin_amdgcn_make_buffer_rsrc((void*)p, (short)0, (int)bytes, 0x00020000); }
DI u64 mix(u64 e) { return e * 0x9E3779B97F4A7C15ull + 0x632BE59BD9B4E019ull; }

template <int OFF> DI void ld128_sc1(u32x4& d, u32 voff, rsrc_t rs, u32 soff) {
    asm volatile("buffer_load_dwordx4 %0, %1, %2, %3 offen offset:%4 sc1" : "=v"(d) : "v"(voff), "s"(rs), "s"(soff), "i"(OFF));
}
template <int OFF> DI void rld128_sc1(u32x4& d, u32 voff, rsrc_t rs, u32 soff) {
    asm volatile("buffer_load_dwordx4 %0, %1, %2, %3 offen offset:%4 sc1" : "+v"(d) : "v"(voff), "s"(rs), "s"(soff), "i"(OFF));
}
DI void ld64_sc1(u32x2& d, u32 voff, rsrc_t rs, u32 soff) {
    asm volatile("buffer_load_dwordx2 %0, %1, %2, %3 offen sc1" : "=v"(d) : "v"(voff), "s"(rs), "s"(soff));
}
template <int OFF> DI void st128_sc1(u32x4 v, u32 voff, rsrc_t rs, u32 soff) {
    asm volatile("buffer_store_dwordx4 %0, %1, %2, %3 offen offset:%4 sc1\n\ts_nop 1" :: "v"(v), "v"(voff), "s"(rs), "s"(soff), "i"(OFF) : "memory");
}
template <int OFF> DI void st64_sc1(u32x2 v, u32 voff, rsrc_t rs, u32 soff) {
    asm volatile("buffer_store_dwordx2 %0, %1, %2, %3 offen offset:%4 sc1" :: "v"(v), "v"(voff), "s"(rs), "s"(soff), "i"(OFF) : "memory");
}
template <int LDSOFF> DI void set_m0(u32 base) { asm volatile("s_add_u32 m0, %0, %1" :: "s"(base), "i"(LDSOFF) : "scc"); }
DI void glds_go(u32 voff, rsrc_t rs, u32 soff) {
    asm volatile("buffer_load_dwordx4 %0, %1, %2 offen lds" :: "v"(voff), "s"(rs), "s"(soff)); }
template <int LDSOFF> DI void glds(u32 base, u32 voff, rsrc_t rs, u32 soff) {
    asm volatile("s_add_u32 m0, %0, %4\n\ts_nop 0\n\tbuffer_load_dwordx4 %1, %2, %3 offen lds"
                 :: "s"(base), "v"(voff), "s"(rs), "s"(soff), "i"(LDSOFF) : "scc");
}
template <int N> DI void wait_vm() { asm volatile("s_waitcnt vmcnt(%0)" :: "i"(N) : "memory"); }
DI void pin(u32x4& a) { asm volatile("" : "+v"(a)); }
DI void pin(u32x2& a) { asm volatile("" : "+v"(a)); }
DI void pin(u32& a) { asm volatile("" : "+v"(a)); }
#define PINE(v, i) do { u32 e_ = (v)[i]; pin(e_); (v)[i] = e_; } while (0)
DI bool all_eq(u32x2 v, u32x2 tag) { return __builtin_amdgcn_ballot_w64((v[0] != tag[0]) | (v[1] != tag[1])) == 0; }

DI void poll(rsrc_t ws, u32 voff, u32x2 tag) {
#pragma unroll 1
    for (int it = 0; it < (1 << 18); ++it) {
        u32x2 v;
        asm volatile("buffer_load_dwordx2 %0, %1, %2, 0 offen sc1\n\ts_waitcnt vmcnt(0)" : "=v"(v) : "v"(voff), "s"(ws) : "memory");
        if (all_eq(v, tag)) break;
        __builtin_amdgcn_s_sleep(1);
    }
}

#define TS(i) do {} while (0)

DI u32 phi2(float a, float b) {
    f32x2 t = {a, b};
    f32x2 y = t * 1.4426950408889634f;
    f32x2 u = t + 1.0f;
    f32x2 r = {vmax(u[0], __builtin_amdgcn_fmed3f(__builtin_amdgcn_exp2f(y[0]), 0.f, 1.f)),
               vmax(u[1], __builtin_amdgcn_fmed3f(__builtin_amdgcn_exp2f(y[1]), 0.f, 1.f))};
    r = r + 1e-6f;
    return pk2(r[0], r[1]);
}

template <bool ISA>
DI void body(const u16* __restrict__ xg, const u16* __restrict__ wg_, const u16* __restrict__ wog, u16* __restrict__ outg,
             char* wsp, int t, int g, char* smem) {
    constexpr const Sched& SC = ISA ? SCHED_A : SCHED_B;
    const int tid = threadIdx.x;
    const int lane = tid & 63;
    const int w = __builtin_amdgcn_readfirstlane(tid >> 6);
    const int gam = lane >> 4, c16 = lane & 15, hc = c16 >> 2;
    const int b = ISA ? 0 : 1;
    const int wg = (b * 16 + t) * 8 + g;
    const u32 lds0 = (u32)(size_t)(LAS char*)smem;
    TS(0);

    u32 cpf;
    {
        u64 pc; u32 len;
        if constexpr (ISA) asm volatile("s_getpc_b64 %0\n.Lfk_pc_a:\n\ts_mov_b32 %1, .Lfk_end_a-.Lfk_pc_a" : "=s"(pc), "=s"(len));
        else               asm volatile("s_getpc_b64 %0\n.Lfk_pc_b:\n\ts_mov_b32 %1, .Lfk_end_b-.Lfk_pc_b" : "=s"(pc), "=s"(len));
        len = len > 65536u ? 0u : len;
        const rsrc_t rsC = mk_rsrc((const void*)pc, len);
        asm volatile("buffer_load_dword %0, %1, %2, 0 offen" : "=v"(cpf) : "v"(lane < 16 ? (u32)((g & 3) + 4 * (lane + 16 * w)) * 128u : OOB), "s"(rsC));
    }
    SB();
    const rsrc_t rsW = mk_rsrc(wg_, DM * 3 * DM * 2);
    const rsrc_t rsX = mk_rsrc(xg + (size_t)(t * 128 + w * 32) * DM, 32 * DM * 2);
    const int rot = (t + g) % 5;
    u32 rck[5];
#pragma unroll
    for (int j = 0; j < 5; ++j) { int c = j + rot; c = c >= 5 ? c - 5 : c; rck[j] = (u32)c; }
    const u32 ldsw = lds0 + (u32)w * 5120u;
    const u32 ldsx = lds0 + XST_OFF + (u32)w * 20480u;
    u32 vx[4];
#pragma unroll
    for (int j = 0; j < 4; ++j) { int i = 8 * (j & 1) + (lane >> 3); vx[j] = (u32)((2 * i + (j >> 1)) * 1280 + (((lane & 7) ^ (i >> 1)) * 16)); }
    auto soffX = [&](auto H) { return rck[H.v >> 1] * 256u + (u32)(H.v & 1) * 128u; };
    auto issueX = [&](auto H) { const u32 so = soffX(H); sfor<0, 4>([&](auto J) { glds<(H.v % 5) * 4096 + J.v * 1024>(ldsx, vx[J.v], rsX, so); }); };
    issueX(IC<0>{});
    SB();
    u32 vw[5];
#pragma unroll
    for (int i = 0; i < 5; ++i) {
        int c = i * 64 + lane;
        int rl = c / 10, ch = c - rl * 10;
        vw[i] = (u32)((rl ^ ((rl >> 1) & 4)) * 3840 + ch * 16);
    }
    const u32 wbase = (u32)w * (32u * 3840u) + (u32)g * 160u;
    auto soffW = [&](auto S) { return rck[st_chunk(S.v)] * (128u * 3840u) + (u32)(st_type(S.v) * 1280) + wbase; };
    auto issueW = [&](auto S) { const u32 so = soffW(S); sfor<0, 5>([&](auto I) { glds<st_slot(S.v) * SLOT_B + I.v * 1024>(ldsw, vw[I.v], rsW, so); }); };
    issueW(IC<0>{});
    const rsrc_t rsWo = mk_rsrc(wog, DM * DM * 2);
    const rsrc_t rsWS = mk_rsrc(wsp, WS_BYTES);
    u16* const outp = outg + (size_t)(t * 128 + w * 32) * (2 * DM) + b * DM + g * 80;
    const rsrc_t rsO = mk_rsrc(outp, 31 * 2 * DM * 2 + 160);
    u32x2 ep; u32 touch;
    {
        const u32 tv = lane == 0 ? (ISA ? (u32)(128 * (1 + t)) : 0u) : lane == 1 ? (ISA ? (u32)(128 * t) : 0u)
                     : lane == 2 ? WS_O + (u32)((b * 16 + t) * 4 + w) * OBLK + 40960u : lane == 3 ? WS_P1 + (u32)wg * 8192u + 8064u : lane == 4 ? WS_O - 128u : OOB;
        ld64_sc1(ep, tv, rsWS, 0u);
        const rsrc_t rsT = mk_rsrc((w & 1) ? (const void*)wog : (const void*)outp, (w & 1) ? DM * DM * 2 : 160);
        asm volatile("buffer_load_dword %0, %1, %2, 0 offen" : "=v"(touch) : "v"((u32)lane * 65536u), "s"(rsT));
    }
    SB();

    u32 vo[5], vo4[5];
#pragma unroll
    for (int i = 0; i < 5; ++i) {
        int c = i * 64 + lane;
        int rl = c / 10, ch = c - rl * 10;
        int k = rl ^ ((rl >> 1) & 4);
        int ga = k >> 3, e = k & 7;
        vo[i] = (u32)((20 * (e >> 2) + 4 * ga + (e & 3)) * 1280 + ch * 16);
        vo4[i] = (u32)((80 * ((g + 2 * w + (e >> 2)) & 7) + 20 * ga + 16 + (e & 3)) * 1280 + ch * 16);
        asm volatile("" : "+v"(vo[i]), "+v"(vo4[i]));
    }
    auto soffO = [&](auto S) {
        constexpr int ck = st_chunk(S.v);
        const u32 row = ck < 4 ? 80u * (u32)((g + 2 * ck + (w >> 1)) & 7) + 40u * (u32)(w & 1) : 0u;
        return row * 1280u + (u32)g * 160u;
    };

    u32 rb[2][2][2];
    {
        int q = (lane >> 2) & 3, p = lane & 3;
#pragma unroll
        for (int tt = 0; tt < 2; ++tt) {
            int row = 8 * gam + 4 * (tt ^ (gam & 1)) + q;
            rb[0][tt][0] = lds0 + (u32)(row * 160 + 8 * p);
            rb[0][tt][1] = lds0 + (u32)(row * 160 + 40 * p + 32);
            rb[1][tt][0] = rb[0][tt][0] + 3 * SLOT_B;
            rb[1][tt][1] = rb[0][tt][1] + 3 * SLOT_B;
            asm volatile("" : "+v"(rb[0][tt][0]), "+v"(rb[0][tt][1]), "+v"(rb[1][tt][0]), "+v"(rb[1][tt][1]));
        }
    }
    u32x2 Bh[2][5][2];
    auto readB = [&](auto N, auto F, auto T) {
        constexpr int n = N.v, s = n >> 2, kk = n & 3, slot = st_slot(s), hf = slot / 3, f = F.v;
        constexpr int off = (slot % 3) * SLOT_B + kk * 5120;
        Bh[n & 1][f][T.v] = f < 4 ? trread(rb[hf][T.v][0] + off + 40 * f) : trread(rb[hf][T.v][1] + off);
    };
    auto readBall = [&](auto N) { sfor<0, 5>([&](auto F) { readB(N, F, IC<0>{}); readB(N, F, IC<1>{}); SB(); }); };
    u32 rx[2];
#pragma unroll
    for (int kk = 0; kk < 2; ++kk) { rx[kk] = ldsx + (u32)(c16 * 128 + (((4 * kk + gam) ^ (c16 >> 1)) * 16)); asm volatile("" : "+v"(rx[kk])); }
    u32x4 xf[2][20];
    auto readX = [&](auto H, auto J) {
        constexpr int h = H.v, p = J.v & 1, kk = J.v >> 1;
        xf[p][h * 2 + kk] = lds_ld<u32x4>(rx[kk] + (h % 5) * 4096 + p * 2048);
    };

    f32x4 acc[2][5];
    const u32 xch = lds0 + XCH_OFF;
    u32 vl = (u32)lane * 16u;
    const u32 myP1 = WS_P1 + (u32)wg * 8192u;
    const u32 obase = WS_O + (u32)((b * 16 + t) * 4 + w) * OBLK;
    u32 vflag = lane < 8 ? vl : OOB;
    u32 vh0 = (ISA && w == 3 && lane >= 32) ? OOB : vl;
    u32 vh1 = (ISA && w == 3) ? OOB : vl;
    u32 f1src = ISA ? WS_F1 + (u32)((t > 0 ? wg - 8 : wg) * 4 + w) * 128u : WS_F1 + (u32)((128 + g) * 4 + w) * 128u + (u32)c16 * 4096u;
    u32 f2src = WS_F2 + (u32)(((b * 16 + t) * 8 + (lane & 7)) * 4 + w) * 128u;
    u32 vst0 = xch + (u32)(w * 5120 + c16 * 320 + gam * 8), vst1 = xch + (u32)(w * 5120 + c16 * 320 + gam * 40 + 32);
    u32 vout[5];
#pragma unroll
    for (int i = 0; i < 5; ++i) { int c = i * 64 + lane; int rl = c / 10; vout[i] = (u32)(rl * 2560 + (c - rl * 10) * 16); asm volatile("" : "+v"(vout[i])); }
    u32x4 on = {0x3F803F80u, 0x3F803F80u, 0x3F803F80u, 0x3F803F80u};
    asm volatile("" : "+v"(vl), "+v"(vflag), "+v"(vh0), "+v"(vh1), "+v"(f1src), "+v"(f2src), "+v"(vst0), "+v"(vst1), "+v"(on));
    u32x2 tag, tagp; u32x4 tag4;
    u32 wpf, vwpf = lane < 50 ? (u32)((((t & 3) * 8 + g) * 4 + w) * 50 + lane) * 128u : OOB;
    asm volatile("" : "+v"(vwpf));

    f32x4 cown, cprv; u32x2 mown, mprv;
    u32 kwr, vwr, krd, vrd;
    u32x4 S1;
    u32 a1m[4], a2m[4], a2n[4], a3m[4], l8, zrd, vgl, swr;
    if constexpr (ISA) {
        const bool hasprev = (w > 0) || (t > 0);
        bool vo_[4], vp_[4];
#pragma unroll
        for (int j = 0; j < 4; ++j) {
            int kk = 4 * gam + j;
            vo_[j] = (kk <= c16) && (kk >= c16 - 7);
            vp_[j] = (kk >= c16 + 9) && hasprev;
            cown[j] = vo_[j] ? 0.f : -1e30f;
            cprv[j] = vp_[j] ? 0.f : -1e30f;
        }
        mown[0] = (vo_[0] ? 0xFFFFu : 0u) | (vo_[1] ? 0xFFFF0000u : 0u);
        mown[1] = (vo_[2] ? 0xFFFFu : 0u) | (vo_[3] ? 0xFFFF0000u : 0u);
        mprv[0] = (vp_[0] ? 0xFFFFu : 0u) | (vp_[1] ? 0xFFFF0000u : 0u);
        mprv[1] = (vp_[2] ? 0xFFFFu : 0u) | (vp_[3] ? 0xFFFF0000u : 0u);
        kwr = xch + (c16 >= 8 ? (u32)(w + 1) * 8192u : 0u) + (u32)(gam * 8 + (c16 & 7)) * 16u;
        vwr = xch + (gam >= 2 ? (u32)(w + 1) * 8192u : 0u) + 4096u + (u32)(lane & 31) * 8u;
        krd = xch + (u32)w * 8192u + (u32)(gam * 8 + (c16 & 7)) * 16u;
        vrd = xch + (u32)w * 8192u + 4096u + (u32)(lane & 31) * 8u;
        asm volatile("" : "+v"(cown), "+v"(cprv), "+v"(mown), "+v"(mprv), "+v"(kwr), "+v"(vwr), "+v"(krd), "+v"(vrd));
    } else {
        const u32 one = ((gam & 1) == (c16 >> 3)) ? ((c16 & 1) ? 0x3F800000u : 0x00003F80u) : 0u;
#pragma unroll
        for (int d = 0; d < 4; ++d) S1[d] = ((c16 >> 1) & 3) == d ? one : 0u;
        vgl = (u32)(lane & 31) * 16u + (u32)(lane >> 5) * (8u * 8u * 8192u);
        swr = xch + 32768u + (u32)(2 * w + (gam & 1)) * 512u + (u32)((gam >> 1) * 16 + c16) * 8u;
        l8 = xch + 32768u + (u32)lane * 8u; zrd = xch + 32768u + (u32)(6 * 512) + (u32)gam * 128u;
        const u32 zb = xch + 32768u + 4096u;
#pragma unroll
        for (int h = 0; h < 4; ++h) {
            a1m[h] = gam == h ? l8 + 4 * 512 : zb;
            a2m[h] = hc == h ? l8 + 5 * 512 : zb;
            a2n[h] = (hc == h && gam == h) ? l8 + 7 * 512 : zb;
            a3m[h] = gam == h ? xch + 32768u + (u32)(7 * 512 + h * 128 + ((h + 1) & 3) * 32) : zb;
            asm volatile("" : "+v"(a1m[h]), "+v"(a2m[h]), "+v"(a2n[h]), "+v"(a3m[h]));
        }
        asm volatile("" : "+v"(S1), "+v"(l8), "+v"(zrd), "+v"(vgl), "+v"(swr));
    }

    u32x4 Kown[2][4], Kprv[2][4], Vop[2][5];
    u32x4 PK[5];
    u32x4 hcp[2];
    u32x2 flg, fl2;
    u32x4 gl[16];

    auto filler = [&](auto M) {
        constexpr int m = M.v;
        if constexpr (m == M_WPF) asm volatile("buffer_load_dword %0, %1, %2, 0 offen" : "=v"(wpf) : "v"(vwpf), "s"(rsWo));
        if constexpr (ISA) {
            if constexpr (m == M_PUBA) {
                hcp[0] = lds_ld<u32x4>(xch + 4 * 8192 + (u32)w * 2048u + vl);
                hcp[1] = lds_ld<u32x4>(xch + 4 * 8192 + (u32)w * 2048u + vl + 1024);
            }
            if constexpr (m == M_PUBA + 4) st128_sc1<0>(hcp[0], vh0, rsWS, myP1 + (u32)w * 2048u);
            if constexpr (m == M_PUBA + 5) st128_sc1<1024>(hcp[1], vh1, rsWS, myP1 + (u32)w * 2048u);
            if constexpr (m >= 295) {
                constexpr int i = m - 295;
                if constexpr (i < 4) { Kprv[i >> 1][(i & 1) * 2] = lds_ld<u32x4>(krd + 2 * i * 512); Kprv[i >> 1][(i & 1) * 2 + 1] = lds_ld<u32x4>(krd + (2 * i + 1) * 512); }
#pragma unroll
                for (int q = 2 * i; q < 2 * i + 2; ++q) { u32x2 v = lds_ld<u32x2>(vrd + q * 256); Vop[q / 5][q % 5][2] = v[0]; Vop[q / 5][q % 5][3] = v[1]; }
            }
        } else {
            if constexpr (m >= M_CHK && m < M_CHK + 16) ld128_sc1<((m - M_CHK) & 1) * 512>(gl[m - M_CHK], vgl, rsWS, WS_P1 + (u32)((16 + ((((m - M_CHK) >> 1) + 2 * t) & 7)) * 8 + g) * 8192u + (u32)w * 1024u);
        }
        if constexpr (m == M_POLL - 1) ld64_sc1(fl2, f2src, rsWS, 0u);
        if constexpr (m == M_POLL) ld64_sc1(flg, f1src, rsWS, 0u);
    };

    auto sync = [&](auto S, auto LG) {
        constexpr int s = S.v;
        static_assert(SC.n_sync[s] >= 0, "W piece not issued yet");
        constexpr int xc = sync_m(s) % 20 == 15 ? xrd_half(sync_m(s) / 5) : -1;
        constexpr int nw = (xc >= 0 && SC.n_x[xc < 0 ? 0 : xc] < SC.n_sync[s]) ? SC.n_x[xc < 0 ? 0 : xc] : SC.n_sync[s];
        SB();
        if constexpr (LG.v) asm volatile("s_waitcnt vmcnt(%0) lgkmcnt(0)\n\ts_barrier" :: "i"(cap63(nw)) : "memory");
        else asm volatile("s_waitcnt vmcnt(%0)\n\ts_barrier" :: "i"(cap63(nw)) : "memory");
        SB();
    };
    auto group = [&](auto M) {
        constexpr int m = M.v, n = m / 5, f = m % 5, s = n >> 2, kk = n & 3, ty = st_type(s), ck = st_chunk(s);
        constexpr bool trans = ty == 0 || (ISA && ty == 1);
        constexpr bool first = ck == 0 && kk == 0;
        constexpr bool pre = (n + 1) % 20 != 0;
        constexpr int xc = xrd_half(n);
        static_assert(xc < 0 || SC.n_x[xc < 0 ? 0 : xc] >= 0, "x piece not issued yet");
        if constexpr (m == sync_m(s)) sync(IC<s>{}, IC<(ISA && s == 14)>{});
        else if constexpr (f == 0 && xc >= 0) { wait_vm<cap63(SC.n_x[xc < 0 ? 0 : xc])>(); SB(); }
        constexpr int vk = SC.kind[m], vi = SC.id[m], vp = SC.piece[m];
        const u32x4 Bw = cat(Bh[n & 1][f][0], Bh[n & 1][f][1]);
        if constexpr (vk == 1) set_m0<st_slot(vi) * SLOT_B + vp * 1024>(ldsw);
        if constexpr (vk == 2) set_m0<(vi % 5) * 4096 + vp * 1024>(ldsx);
        mml<trans, first>(acc[0][f], xf[0][ck * 4 + kk], Bw);
        SB();
        if constexpr (pre) readB(IC<n + 1>{}, IC<f>{}, IC<0>{});
        if constexpr (vk == 1) {
            if constexpr (st_type(vi) == 3) glds_go(st_chunk(vi) < 4 ? vo[vp] : vo4[vp], rsWo, soffO(IC<vi>{}));
            else glds_go(vw[vp], rsW, soffW(IC<vi>{}));
        }
        if constexpr (vk == 2) glds_go(vx[vp], rsX, soffX(IC<vi>{}));
        if constexpr (xc >= 0 && f < 4) readX(IC<xc>{}, IC<f>{});
        filler(M);
        SB();
        mml<trans, first>(acc[1][f], xf[1][ck * 4 + kk], Bw);
        SB();
        if constexpr (pre) readB(IC<n + 1>{}, IC<f>{}, IC<1>{});
        SB();
    };

    SB();
    issueX(IC<1>{}); issueW(IC<1>{}); issueX(IC<2>{});
    SB();
    wait_vm<SC.n_init>();
    sfor<0, 4>([&](auto J) { readX(IC<0>{}, J); });
    asm volatile("s_barrier" ::: "memory");
    TS(1);
    readBall(IC<0>{});

    sfor<0, 100>(group);
    POST_MFMA();
    {
        pin(ep); pin(touch); pin(cpf);
        u64 a = mix(((u64)(u32)__builtin_amdgcn_readlane(ep[1], 0) << 32) | (u32)__builtin_amdgcn_readlane(ep[0], 0));
        u64 c = mix(((u64)(u32)__builtin_amdgcn_readlane(ep[1], 1) << 32) | (u32)__builtin_amdgcn_readlane(ep[0], 1));
        tag = (u32x2){(u32)a, (u32)(a >> 32)}; tagp = (u32x2){(u32)c, (u32)(c >> 32)};
        tag4 = cat(tag, tag); pin(tag4);
    }
    if constexpr (ISA) {
#pragma unroll
        for (int p = 0; p < 2; ++p)
#pragma unroll
            for (int h = 0; h < 4; ++h) {
                f32x4 k4 = acc[p][4]; asm volatile("" : "+v"(k4));
                Kown[p][h] = cat(pk4(acc[p][h]), pk4(k4)); pin(Kown[p][h]);
            }
    } else {
#pragma unroll
        for (int f = 0; f < 5; ++f) {
            PK[f] = (u32x4){phi2(acc[0][f][0], acc[0][f][1]), phi2(acc[0][f][2], acc[0][f][3]), phi2(acc[1][f][0], acc[1][f][1]), phi2(acc[1][f][2], acc[1][f][3])};
            pin(PK[f]);
        }
    }
    sync(IC<4>{}, IC<0>{});
    readBall(IC<20>{});
    if constexpr (ISA) {
#pragma unroll
        for (int i = 0; i < 8; ++i) lds_st(kwr + i * 512, Kown[i >> 2][i & 3]);
        SB();
    }
    sfor<100, 200>(group);
    POST_MFMA();
    TS(2);
    if constexpr (ISA) {
#pragma unroll
        for (int i = 0; i < 10; ++i) {
            u32x2 v = pk4(acc[i / 5][i % 5]);
            Vop[i / 5][i % 5][0] = v[0]; Vop[i / 5][i % 5][1] = v[1];
            lds_st(vwr + i * 256, v);
        }
        sync(IC<9>{}, IC<1>{});
        readBall(IC<40>{});
    } else {
        u32x4 VV[5];
        f32x4 G[8], d1[4], d2[4], zz[4], d3, z4;
        VV[4] = cat(pk4(acc[0][4]), pk4(acc[1][4]));
        PRE_MFMA();
        sfor<0, 4>([&](auto H) {
            constexpr int h = H.v;
            mv0(zz[h], PK[h], on);
            SB();
            VV[h][0] = pk2(acc[0][h][0], acc[0][h][1]); VV[h][1] = pk2(acc[0][h][2], acc[0][h][3]);
            SB();
            mv0(d2[h], PK[h], VV[4]);
            SB();
            VV[h][2] = pk2(acc[1][h][0], acc[1][h][1]); VV[h][3] = pk2(acc[1][h][2], acc[1][h][3]);
            SB();
        });
        mv0(d3, PK[4], VV[4]);
        mv0(z4, PK[4], on);
        SB();
        sfor<0, 4>([&](auto H) {
            constexpr int h = H.v;
            mv0(d1[h], PK[4], VV[h]);
            SB();
            G[6][h] = sel4(hc, zz[0][h], zz[1][h], zz[2][h], zz[3][h]);
            SB();
            mv0(G[h], PK[h], VV[h]);
            SB();
            G[5][h] = sel4(hc, d2[0][h], d2[1][h], d2[2][h], d2[3][h]);
            SB();
        });
#pragma unroll
        for (int j = 0; j < 4; ++j) G[7][j] = gam == hc ? d3[j] : z4[j];
        const u32 wr = xch + (u32)w * 8192u + vl;
        lds_st(wr + 5 * 1024, G[5]); lds_st(wr + 6 * 1024, G[6]); lds_st(wr + 7 * 1024, G[7]);
        lds_st(l8 + 4096u, (u32x2){0u, 0u});
        POST_MFMA();
#pragma unroll
        for (int i = 0; i < 4; ++i) lds_st(wr + i * 1024, G[i]);
#pragma unroll
        for (int j = 0; j < 4; ++j) G[4][j] = sel4(gam, d1[0][j], d1[1][j], d1[2][j], d1[3][j]);
        lds_st(wr + 4 * 1024, G[4]);
        SB();
        asm volatile("s_waitcnt vmcnt(%0) lgkmcnt(0)\n\ts_barrier" :: "i"(cap63(SC.n_sync[9] - 1)) : "memory");
        SB();
        f32x4 s[2];
#pragma unroll
        for (int i = 0; i < 2; ++i) {
            const u32 rd = xch + (u32)w * 2048u + vl + i * 1024;
            s[i] = (lds_ld<f32x4>(rd) + lds_ld<f32x4>(rd + 8192)) + (lds_ld<f32x4>(rd + 16384) + lds_ld<f32x4>(rd + 24576));
        }
        sfor<0, 5>([&](auto F) { readB(IC<40>{}, F, IC<0>{}); readB(IC<40>{}, F, IC<1>{}); });
        st128_sc1<0>(cat(pk4(s[0]), pk4(s[1])), vl, rsWS, myP1 + (u32)w * 1024u);
        SB();
    }
    TS(3);

    sfor<200, M_FLAG>(group);
    wait_vm<SC.pos[M_FLAG] - 1 - SC.pos[ISA ? M_PUBA + 6 : 200]>();
    st128_sc1<0>(tag4, vflag, rsWS, WS_F1 + (u32)(wg * 4 + w) * 128u);
    SB();
    sfor<M_FLAG, M_CHK>(group);
    TS(4);
    {
        wait_vm<SC.pos[M_CHK] - xpre(ISA, M_CHK) - SC.pos[M_POLL + 1]>();
        pin(flg); pin(fl2); pin(wpf);
        if constexpr (ISA) {
            if (t > 0 && !all_eq(flg, tagp)) poll(rsWS, f1src, tagp);
            SB();
            const u32 so = t > 0 ? myP1 - 8u * 8192u + (u32)w * 2048u : OOB;
            ld128_sc1<0>(hcp[0], vh0, rsWS, so);
            ld128_sc1<1024>(hcp[1], vh1, rsWS, so);
        } else {
            if (!all_eq(flg, tag)) poll(rsWS, f1src, tag);
        }
        SB();
    }
    TS(5);
    sfor<M_CHK, M_PARK>(group);
    if constexpr (ISA) {
        wait_vm<SC.pos[M_PARK] - SC.pos[M_CHK]>();
        pin(hcp[0]); pin(hcp[1]);
        lds_st(xch + (u32)w * 2048u + vl, hcp[0]);
        lds_st(xch + (u32)w * 2048u + vl + 1024, hcp[1]);
        SB();
    }
    sfor<M_PARK, 300>(group);
    auto wopiece = [&](auto J) {
        constexpr int st = 15 + NOIN + J.v / 5, i = J.v % 5;
        if constexpr (st < 20) { SB(); glds<st_slot(st) * SLOT_B + i * 1024>(ldsw, st_chunk(st) < 4 ? vo[i] : vo4[i], rsWo, soffO(IC<st>{})); SB(); }
    };
    static_assert(NOIN >= 3, "at most ten pieces are placed in the epilogue");
    POST_MFMA();
    TS(6);

    u32x4 of[2][20];
    u32x4 hq[8];
    auto publish = [&](auto P, u32x2 h4) {
        constexpr int p = P.v;
        st128_sc1<0>(of[p][0], vl, rsWS, obase + (u32)p * 16384u + (u32)g * 2048u);
        st128_sc1<1024>(of[p][1], vl, rsWS, obase + (u32)p * 16384u + (u32)g * 2048u);
        hq[0][2 * p] = h4[0]; hq[0][2 * p + 1] = h4[1];
        if constexpr (p == 1) st128_sc1<0>(hq[0], vl, rsWS, obase + 32768u + (u32)g * 1024u);
    };
    if constexpr (ISA) {
        u32x4 Qf[2][4];
        f32x4 so[2][4], sp[2][4];
        sfor<0, 2>([&](auto P) {
            constexpr int p = P.v;
            u32x2 q4 = pk4(acc[p][4]);
            sfor<0, 4>([&](auto H) {
                constexpr int h = H.v, i = p * 4 + h;
                Qf[p][h] = cat(pk4(acc[p][h]), (u32x2){gam == h ? q4[0] : 0u, gam == h ? q4[1] : 0u});
                SB();
                if constexpr (i % 2 == 0) wopiece(IC<i / 2>{});
                if constexpr (i > 0) {
                    mvc(so[(i - 1) >> 2][(i - 1) & 3], Kown[(i - 1) >> 2][(i - 1) & 3], Qf[(i - 1) >> 2][(i - 1) & 3], cown);
                    mvc(sp[(i - 1) >> 2][(i - 1) & 3], Kprv[(i - 1) >> 2][(i - 1) & 3], Qf[(i - 1) >> 2][(i - 1) & 3], cprv);
                    SB();
                }
            });
        });
        mvc(so[1][3], Kown[1][3], Qf[1][3], cown);
        mvc(sp[1][3], Kprv[1][3], Qf[1][3], cprv);
        SB();
        asm volatile("s_nop 2" :: "v"(cown), "v"(cprv));
        SB();
        const float cs = 0.22360679774997896f * 1.4426950408889634f;
        f32x4 oh[2][4], o4[2][4], dn[2][4];
        u32x4 Pf[2][4];
        auto pv = [&](auto P, auto I) {
            constexpr int p = P.v, h = I.v / 3, k = I.v % 3;
            SB();
            if constexpr (k == 0) mv0(dn[p][h], on, Pf[p][h]); else if constexpr (k == 1) mv0(oh[p][h], Vop[p][h], Pf[p][h]); else mv0(o4[p][h], Vop[p][4], Pf[p][h]);
            SB();
        };
        sfor<0, 2>([&](auto P) {
            constexpr int p = P.v;
            f32x4 L[4]; float mx[4];
            sfor<0, 4>([&](auto H) {
                constexpr int h = H.v;
#pragma unroll
                for (int j = 0; j < 4; ++j) L[h][j] = vmax(so[p][h][j], sp[p][h][j]);
                mx[h] = vmax(vmax(vmax(L[h][0], L[h][1]), L[h][2]), L[h][3]);
                if constexpr (p == 0 && h % 2 == 0) wopiece(IC<4 + h / 2>{});
                if constexpr (p == 1) pv(IC<0>{}, IC<h>{});
            });
            swap16(mx[0], mx[1]); swap16(mx[2], mx[3]);
            float xa = vmax(mx[0], mx[1]), xb = vmax(mx[2], mx[3]);
            if constexpr (p == 1) pv(IC<0>{}, IC<4>{});
            swap32(xa, xb);
            float z0 = vmax(xa, xb) * -cs, z1 = z0;
            if constexpr (p == 1) pv(IC<0>{}, IC<5>{});
            swap32(z0, z1);
            float m0 = z0, m1 = z0, m2 = z1, m3 = z1;
            if constexpr (p == 1) pv(IC<0>{}, IC<6>{});
            swap16(m0, m1); swap16(m2, m3);
            const float mc[4] = {m0, m1, m2, m3};
            if constexpr (p == 1) pv(IC<0>{}, IC<7>{});
            sfor<0, 4>([&](auto H) {
                constexpr int h = H.v;
                u32 p0 = pk2(__builtin_amdgcn_exp2f(__builtin_fmaf(L[h][0], cs, mc[h])), __builtin_amdgcn_exp2f(__builtin_fmaf(L[h][1], cs, mc[h])));
                u32 p1 = pk2(__builtin_amdgcn_exp2f(__builtin_fmaf(L[h][2], cs, mc[h])), __builtin_amdgcn_exp2f(__builtin_fmaf(L[h][3], cs, mc[h])));
                Pf[p][h] = (u32x4){p0 & mown[0], p1 & mown[1], p0 & mprv[0], p1 & mprv[1]};
                if constexpr (p == 0) wopiece(IC<6 + h>{});
                if constexpr (p == 1) pv(IC<0>{}, IC<8 + h>{});
            });
            SB();
        });
        sfor<0, 2>([&](auto P) {
            constexpr int p = P.v;
            if constexpr (p == 1) POST_MFMA();
            float r[4]; u32x2 o2[4];
            sfor<0, 4>([&](auto H) {
                constexpr int h = H.v;
                if constexpr (p == 0) { pv(IC<1>{}, IC<2 * h>{}); }
                asm volatile("" : "+v"(dn[p][h]));
                r[h] = __builtin_amdgcn_rcpf(dn[p][h][0]);
                f32x4 t4 = {oh[p][h][0] * r[h], oh[p][h][1] * r[h], oh[p][h][2] * r[h], oh[p][h][3] * r[h]};
                if constexpr (p == 0) { pv(IC<1>{}, IC<2 * h + 1>{}); }
                o2[h] = pk4(t4);
            });
            f32x4 s4;
            sfor<0, 4>([&](auto J) {
                if constexpr (p == 0) { pv(IC<1>{}, IC<8 + J.v>{}); }
                s4[J.v] = sel4(gam, o4[p][0][J.v], o4[p][1][J.v], o4[p][2][J.v], o4[p][3][J.v]);
            });
            float r4 = sel4(gam, r[0], r[1], r[2], r[3]);
            of[p][0] = cat(o2[0], o2[1]); of[p][1] = cat(o2[2], o2[3]);
            publish(P, pk4(s4 * r4));
            SB();
        });
    } else {
        u32x4 Qf[2][4];
        f32x4 sm[2];
        u32x2 q4[2];
        u32x4 A1[4], A2[4], A3[4];
        f32x4 nh[2][4], n4[2], dn[2][4];
        auto nd = [&](auto P, auto I) {
            constexpr int p = P.v, h = I.v / 3, k = I.v % 3;
            SB();
            if constexpr (k == 0) mv0(dn[p][h], A3[h], Qf[p][h]); else if constexpr (k == 1) mv0(nh[p][h], A1[h], Qf[p][h]);
            else if constexpr (h == 0) mv0(n4[p], A2[0], Qf[p][0]); else mva(n4[p], A2[h], Qf[p][h]);
            SB();
        };
        auto red = [&](auto J) {
            constexpr int j = 2 * J.v;
            SB();
            wait_vm<cap63(SC.pos[NMG] - SC.pos[M_CHK + j + 2])>(); pin(gl[j]); pin(gl[j + 1]); SB();
            if constexpr (j == 0) { mv0(sm[0], S1, gl[0]); mv0(sm[1], S1, gl[1]); } else { mva(sm[0], S1, gl[j]); mva(sm[1], S1, gl[j + 1]); }
            SB();
        };
        sfor<0, 10>([&](auto I) {
            constexpr int i = I.v, p = i / 5, f = (i % 5 + 4) % 5;
            u32x2 v_;
            if constexpr (i < 2) { red(IC<4 * i>{}); red(IC<4 * i + 1>{}); }
            if constexpr (i >= 6) nd(IC<0>{}, IC<3 * (i - 6)>{});
            v_[0] = phi2(acc[p][f][0], acc[p][f][1]);
            if constexpr (i < 2) { PINE(v_, 0); red(IC<4 * i + 2>{}); red(IC<4 * i + 3>{}); }
            if constexpr (i >= 6) { PINE(v_, 0); nd(IC<0>{}, IC<3 * (i - 6) + 1>{}); nd(IC<0>{}, IC<3 * (i - 6) + 2>{}); }
            v_[1] = phi2(acc[p][f][2], acc[p][f][3]);
            pin(v_);
            if constexpr (f == 4) q4[p] = v_; else { Qf[p][f] = cat(v_, q4[p]); pin(Qf[p][f]); }
            SB();
            if constexpr (i == 1) {
                lds_st(swr, pk4(sm[0]));
                lds_st(swr + 256u, pk4(sm[1]));
                SB();
            }
            if constexpr (i == 3) {
                asm volatile("s_waitcnt lgkmcnt(0)\n\ts_barrier" ::: "memory");
#pragma unroll
                for (int h = 0; h < 4; ++h) {
                    A3[h] = cat(lds_ld<u32x2>(zrd + h * 32), lds_ld<u32x2>(a3m[h]));
                    A1[h] = cat(lds_ld<u32x2>(l8 + h * 512), lds_ld<u32x2>(a1m[h]));
                    A2[h] = cat(lds_ld<u32x2>(a2m[h]), lds_ld<u32x2>(a2n[h]));
                    SB();
                }
            }
            if constexpr (i >= 2) wopiece(IC<i - 2>{});
            if constexpr (i == 4 || i == 5) wopiece(IC<i + 4>{});
        });
        PRE_MFMA();
        sfor<0, 2>([&](auto P) {
            constexpr int p = P.v;
            if constexpr (p == 1) POST_MFMA();
            float r[4]; u32x2 o2[4];
            sfor<0, 4>([&](auto H) {
                constexpr int h = H.v;
                if constexpr (p == 0) { nd(IC<1>{}, IC<3 * h>{}); }
                asm volatile("" : "+v"(dn[p][h]));
                r[h] = __builtin_amdgcn_rcpf(dn[p][h][0] + 1e-6f);
                if constexpr (p == 0) { nd(IC<1>{}, IC<3 * h + 1>{}); }
                f32x4 t4 = {nh[p][h][0] * r[h], nh[p][h][1] * r[h], nh[p][h][2] * r[h], nh[p][h][3] * r[h]};
                if constexpr (p == 0) { nd(IC<1>{}, IC<3 * h + 2>{}); }
                o2[h] = pk4(t4);
            });
            float r4 = sel4(gam, r[0], r[1], r[2], r[3]);
            of[p][0] = cat(o2[0], o2[1]); of[p][1] = cat(o2[2], o2[3]);
            publish(P, pk4(n4[p] * r4));
            SB();
        });
    }
    TS(7);
    wait_vm<0>();
    st128_sc1<0>(tag4, vflag, rsWS, WS_F2 + (u32)(wg * 4 + w) * 128u);
    asm volatile("s_barrier" ::: "memory");
    TS(8);
#define OAHEAD 5
    auto oload = [&](auto K, auto P) {
        constexpr int k = K.v;
        if constexpr (k < 16) ld128_sc1<(k & 1) * 1024>(of[P.v][k], vl, rsWS, obase + (u32)P.v * 16384u + (u32)((g + k / 2) & 7) * 2048u);
        else if constexpr (k > 16 || P.v == 1) ld128_sc1<0>(hq[2 * (k - 16) + P.v], vl, rsWS, obase + 32768u + (u32)((g + 2 * (k - 16) + P.v) & 7) * 1024u);
    };
    auto kstep = [&](auto K) {
        constexpr int k = K.v, n = 60 + k;
        sfor<0, 5>([&](auto F) {
            constexpr int f = F.v;
            const u32x4 Bw = cat(Bh[n & 1][f][0], Bh[n & 1][f][1]);
            if constexpr (k == 0) mv0(acc[0][f], Bw, of[0][k]); else mva(acc[0][f], Bw, of[0][k]);
            SB();
            if constexpr (k < 19) readB(IC<n + 1>{}, F, IC<0>{});
            if constexpr (k >= 2 && k + OAHEAD < 20 && (f == 1 || f == 3)) oload(IC<k + OAHEAD>{}, IC<(f >> 1)>{});
            SB();
            if constexpr (k == 0) mv0(acc[1][f], Bw, of[1][k]); else mva(acc[1][f], Bw, of[1][k]);
            SB();
            if constexpr (k < 19) readB(IC<n + 1>{}, F, IC<1>{});
            SB();
        });
    };
    readBall(IC<60>{});
    sfor<0, 2>(kstep);
    poll(rsWS, f2src, tag);
    TS(9);
    sfor<2, 2 + OAHEAD>([&](auto K) { oload(K, IC<0>{}); oload(K, IC<1>{}); });
    SB();
    sfor<2, 20>([&](auto K) {
        constexpr int k = K.v, last = k + OAHEAD - 1 < 19 ? k + OAHEAD - 1 : 19;
        wait_vm<2 * (last - k) - ((k < 16 && last >= 16) ? 1 : 0)>();
        if constexpr (k < 16) { pin(of[0][k]); pin(of[1][k]); }
        else {
            u32x4& a = hq[2 * (k - 16)]; u32x4& c = hq[2 * (k - 16) + 1];
            pin(a); pin(c);
            of[0][k] = (u32x4){a[0], a[1], c[0], c[1]}; of[1][k] = (u32x4){a[2], a[3], c[2], c[3]};
            pin(of[0][k]); pin(of[1][k]);
            PRE_MFMA();
        }
        SB();
        kstep(K);
    });
    POST_MFMA();
    TS(10);
#pragma unroll
    for (int p = 0; p < 2; ++p) {
#pragma unroll
        for (int f = 0; f < 4; ++f) lds_st(vst0 + (p * 160 + f * 40), pk4(acc[p][f]));
        lds_st(vst1 + p * 160, pk4(acc[p][4]));
    }
#pragma unroll
    for (int i = 0; i < 5; ++i)
        __builtin_amdgcn_raw_buffer_store_b128(lds_ld<u32x4>(xch + (u32)w * 5120u + vl + i * 1024), rsO, vout[i], 0, OAUX);
    if (g == 0 && w == 0) {
        if constexpr (ISA) {
            if (t > 0) st128_sc1<0>(cat(tagp, tagp), vflag, rsWS, WS_EPOCH + (u32)(128 * t));
            if (t == 15) st128_sc1<0>(tag4, vflag, rsWS, WS_EPOCH + (u32)(128 * 16));
        } else {
            if (t == 0) st128_sc1<0>(tag4, vflag, rsWS, WS_EPOCH);
        }
    }
    TS(11);
    if constexpr (ISA) asm volatile(".Lfk_end_a:"); else asm volatile(".Lfk_end_b:");
}

__global__ __launch_bounds__(256, 1) void fused_kernel(const u16* __restrict__ xa, const u16* __restrict__ wa, const u16* __restrict__ woa,
                                                       const u16* __restrict__ xb, const u16* __restrict__ wb, const u16* __restrict__ wob,
                                                       u16* __restrict__ out, char* ws) {
    __shared__ __attribute__((aligned(16))) char smem[LDS_B];
    const int x = blockIdx.x & 7, idx = blockIdx.x >> 3;
    const int t = (x & 3) * 4 + (idx >> 3), g = idx & 7;
#define ASEL 0xF0
    const bool isa = (ASEL >> x) & 1;
    const u16* xp = isa ? xa : xb; const u16* wp = isa ? wa : wb; const u16* wop = isa ? woa : wob;
    asm volatile("" : "+s"(xp), "+s"(wp), "+s"(wop), "+s"(out), "+s"(ws));
    if (isa) body<true>(xp, wp, wop, out, ws, t, g, smem);
    else     body<false>(xp, wp, wop, out, ws, t, g, smem);
}

extern "C" void kernel_launch(void* const* d_in, const int* in_sizes, int n_in,
                              void* d_out, int out_size, void* d_ws, size_t ws_size,
                              hipStream_t stream) {
    fused_kernel<<<256, 256, 0, stream>>>((const u16*)d_in[0], (const u16*)d_in[1], (const u16*)d_in[2],
                                          (const u16*)d_in[3], (const u16*)d_in[4], (const u16*)d_in[5],
                                          (u16*)d_out, (char*)d_ws);
}
```

```cpp
#include <hip/hip_runtime.h>
#include <stdint.h>

typedef unsigned short u16;
typedef unsigned int u32;
typedef unsigned long long u64;
typedef __attribute__((ext_vector_type(4))) short s16x4;
typedef __attribute__((ext_vector_type(2))) __bf16 bf16x2;
typedef __attribute__((ext_vector_type(4))) float f32x4;
typedef __attribute__((ext_vector_type(2))) float f32x2;
typedef __attribute__((ext_vector_type(4))) u32 u32x4;
typedef __attribute__((ext_vector_type(2))) u32 u32x2;
typedef __amdgpu_buffer_rsrc_t rsrc_t;
#define LAS __attribute__((address_space(3)))
#define GAS __attribute__((address_space(1)))
#define DI __device__ __forceinline__
#define HD __host__ __device__
#define SB() __builtin_amdgcn_sched_barrier(0)

#define OAUX 2
#define XMAP 2

constexpr int DM = 640;
constexpr int SLOT_B = 20480;
constexpr int XST_OFF = 4 * SLOT_B;
constexpr int XCH_OFF = 6 * SLOT_B;
constexpr int LDS_B = 163840;
constexpr u32 OOB = 0x7FFFFF00u;

constexpr u32 WS_EPOCH = 0;
constexpr u32 WS_F1 = 4096;
constexpr u32 WS_F2 = WS_F1 + 256 * 4 * 128;
constexpr u32 WS_P1 = WS_F2 + 256 * 4 * 128;
constexpr u32 WS_O = WS_P1 + 256 * 8192;
constexpr u32 OBLK = 2 * 20480 + 128;
constexpr u32 WS_TS = WS_O + 2 * 16 * 4 * OBLK;
constexpr u32 WS_TS2 = WS_TS + 256 * 4 * 16 * 8;
constexpr u32 WS_BYTES = WS_TS2 + 256 * 4 * 64 * 4;

constexpr int NST = 20;
HD constexpr int st_type(int s)  { return s < 5 ? 1 : s < 10 ? 2 : s < 15 ? 0 : 3; }
HD constexpr int st_chunk(int s) { return s % 5; }
HD constexpr int st_slot(int s)  { return s < 8 ? s % 4 : (s - 4) % 6; }
HD constexpr int sync_mb(bool isa, int s) { return (isa ? s == 9 : s == 4) ? 20 * s + 20 : 20 * s + 15; }
HD constexpr int xrd_half(int n) { return (n & 1) && n < 19 ? (n + 1) / 2 : -1; }

constexpr int NMG = 300;
#ifndef M_WPF
#define M_WPF 141
#endif
constexpr int M_PUBA = 200;
#define MA_FLAG 228
#define MA_POLL 250
#define MA_CHK 272
#define MB_FLAG 246
#define MB_POLL 264
#define MB_CHK 284
constexpr int M_PUBB = 224;
HD constexpr int m_flag(bool isa) { return isa ? MA_FLAG : MB_FLAG; }
HD constexpr int m_poll(bool isa) { return isa ? MA_POLL : MB_POLL; }
HD constexpr int m_chk(bool isa) { return isa ? MA_CHK : MB_CHK; }
constexpr int M_PARK = 293;
#define LOOKA 50
#define OSPACE 3
#define OSPACEB 2
#define RI 6
#define SPV 2
#define SPQ0 2
#ifndef M_Q1
#define M_Q1 180
#endif
#define SPQ 3
#define NOIN 4
struct Sched {
    int kind[NMG], id[NMG], piece[NMG];
    int pos[NMG + 1];
    int n_init, n_x[10], n_sync[NST];
    int left_stage;
};
HD constexpr int xpre(bool isa, int m) { return m == m_flag(isa) ? 1 : (isa && m == m_chk(isa)) ? 2 : 0; }
HD constexpr int xop(bool isa, int m) {
    if (m == M_WPF) return 1;
    if (!isa && m >= m_chk(isa) && m < m_chk(isa) + 16) return 1;
    if (!isa && m == M_PUBB) return 1;
    if (isa && m >= M_PUBA + 4 && m < M_PUBA + 6) return 1;
    if (m == m_poll(isa) || m == m_poll(isa) - 1) return 1;
    return 0;
}
HD constexpr Sched make_sched(bool isa) {
    Sched r{};
    int uk[32] = {}, ui[32] = {}, ua[32] = {}, us[32] = {}; int nu = 0;
    auto add = [&](int k, int i, int a, int sp) { uk[nu] = k; ui[nu] = i; ua[nu] = a; us[nu] = sp; ++nu; };
    auto mx = [](int x, int y) { return x > y ? x : y; };
    auto sync_m = [&](int s) { return sync_mb(isa, s); };
    auto xav = [&](int h) { return mx(h < 5 ? 0 : h == 5 ? 5 : 5 * (2 * (h - 5) + 1), 5 * (2 * h - 1) - LOOKA); };
    auto wfree = [&](int q) { return q < 4 ? 0 : q < 8 ? sync_m(q - 3) : q < 10 ? sync_m(4) : sync_m(q - 5); };
    auto wav = [&](int q) { return mx(wfree(q), sync_m(q - 1) - LOOKA); };
    add(2, 3, xav(3), 1);
    for (int q = 2; q < 5; ++q) { add(1, q, wav(q), 1); add(2, 2 * q, xav(2 * q), 1); add(2, 2 * q + 1, xav(2 * q + 1), 1); }
    add(1, 5, wav(5), 1); add(1, 6, wav(6), 1);
    for (int q = 7; q < 10; ++q) add(1, q, wfree(q), SPV);
    add(1, 10, wfree(10), SPQ0); add(1, 11, mx(wfree(11), M_Q1), SPQ);
    for (int q = 12; q < 15; ++q) add(1, q, wfree(q), SPQ);
    for (int q = 15; q < 15 + NOIN; ++q) add(1, q, wfree(q), isa ? OSPACE : OSPACEB);
    int endW[NST] = {}, endX[10] = {};
    int pos = 0;
    pos += 4; endX[0] = pos; pos += 5; endW[0] = pos; pos += 2; pos += 4; endX[1] = pos;
    pos += 5; endW[1] = pos; pos += 4; endX[2] = pos;
    r.n_init = pos - endW[0];
    int u = 0, pc = 0, nextok = 0;
    for (int m = 0; m < NMG; ++m) {
        pos += xpre(isa, m);
        r.pos[m] = pos;
        if (m % 5 == 0 && xrd_half(m / 5) >= 0) r.n_x[xrd_half(m / 5)] = endX[xrd_half(m / 5)] ? pos - endX[xrd_half(m / 5)] : -1000;
        for (int s = 0; s < 15; ++s) if (sync_m(s) == m) r.n_sync[s] = s == 14 ? 63 : endW[s + 1] ? pos - endW[s + 1] : -1000;
        if (u < nu && m >= ua[u] && m >= nextok) {
            r.kind[m] = uk[u]; r.id[m] = ui[u]; r.piece[m] = pc;
            ++pos; ++pc; nextok = m + us[u];
            int np = uk[u] == 1 ? 5 : 4;
            if (pc == np) { if (uk[u] == 1) endW[ui[u]] = pos; else endX[ui[u]] = pos; pc = 0; ++u; }
        }
        pos += xop(isa, m);
    }
    r.pos[NMG] = pos;
    r.left_stage = u < nu ? ui[u] : NST;
    return r;
}
constexpr Sched SCHED_A = make_sched(true), SCHED_B = make_sched(false);
static_assert(SCHED_A.left_stage == NST && SCHED_B.left_stage == NST, "all LDS-DMA pieces must be placed");
HD constexpr int cap63(int n) { return n > 63 ? 63 : n; }

template <int V> struct IC { static constexpr int v = V; };
template <int I, int N, class F> DI void sfor(F&& f) { if constexpr (I < N) { f(IC<I>{}); sfor<I + 1, N>(f); } }

template <bool TRANS, int FIRST> DI void mml(f32x4& d, const u32x4& x, const u32x4& w) {
    if constexpr (FIRST == 2) {
        if constexpr (TRANS) asm volatile("v_mfma_f32_16x16x32_bf16 %0, %2, %1, 1.0" : "=&v"(d) : "a"(x), "v"(w));
        else                 asm volatile("v_mfma_f32_16x16x32_bf16 %0, %1, %2, 1.0" : "=&v"(d) : "a"(x), "v"(w));
    } else if constexpr (FIRST == 1) {
        if constexpr (TRANS) asm volatile("v_mfma_f32_16x16x32_bf16 %0, %2, %1, 0" : "=&v"(d) : "a"(x), "v"(w));
        else                 asm volatile("v_mfma_f32_16x16x32_bf16 %0, %1, %2, 0" : "=&v"(d) : "a"(x), "v"(w));
    } else {
        if constexpr (TRANS) asm volatile("v_mfma_f32_16x16x32_bf16 %0, %2, %1, %0" : "+v"(d) : "a"(x), "v"(w));
        else                 asm volatile("v_mfma_f32_16x16x32_bf16 %0, %1, %2, %0" : "+v"(d) : "a"(x), "v"(w));
    }
}
DI void mv0(f32x4& d, const u32x4& a, const u32x4& b) { asm volatile("v_mfma_f32_16x16x32_bf16 %0, %1, %2, 0" : "=&v"(d) : "v"(a), "v"(b)); }
DI void mva(f32x4& d, const u32x4& a, const u32x4& b) { asm volatile("v_mfma_f32_16x16x32_bf16 %0, %1, %2, %0" : "+v"(d) : "v"(a), "v"(b)); }
DI void mvc(f32x4& d, const u32x4& a, const u32x4& b, const f32x4& c) { asm volatile("v_mfma_f32_16x16x32_bf16 %0, %1, %2, %3" : "=&v"(d) : "v"(a), "v"(b), "v"(c)); }
#define PRE_MFMA()  do { SB(); asm volatile("s_nop 1"); SB(); } while (0)
#define POST_MFMA() do { SB(); asm volatile("s_nop 7"); SB(); } while (0)

DI u32 pk2(float a, float b) { f32x2 v = {a, b}; return __builtin_bit_cast(u32, __builtin_convertvector(v, bf16x2)); }
DI u32x2 pk4(f32x4 v) { u32x2 r = {pk2(v[0], v[1]), pk2(v[2], v[3])}; return r; }
DI u32x4 cat(u32x2 a, u32x2 b) { u32x4 r = {a[0], a[1], b[0], b[1]}; return r; }
DI u32x2 trread(u32 addr) {
    return __builtin_bit_cast(u32x2, __builtin_amdgcn_ds_read_tr16_b64_v4i16((LAS s16x4*)(size_t)addr)); }
template <class T> DI T lds_ld(u32 addr) { return *(LAS T*)(size_t)addr; }
template <class T> DI void lds_st(u32 addr, T v) { *(LAS T*)(size_t)addr = v; }
DI float vmax(float a, float b) { return __builtin_elementwise_maximum(a, b); }
DI float sel4(int i, float a, float b, float c, float d) { float r = a; r = i == 1 ? b : r; r = i == 2 ? c : r; r = i == 3 ? d : r; return r; }
DI u32 umax(u32 a, u32 b) { return __builtin_elementwise_max(a, b); }
DI u32 umax4(u32 m, u32x4 v) { return umax(umax(umax(umax(m, v[0]), v[1]), v[2]), v[3]); }
DI void swap16(float& a, float& b) { auto r = __builtin_amdgcn_permlane16_swap(__float_as_uint(a), __float_as_uint(b), false, false); a = __uint_as_float(r[0]); b = __uint_as_float(r[1]); }
DI void swap32(float& a, float& b) { auto r = __builtin_amdgcn_permlane32_swap(__float_as_uint(a), __float_as_uint(b), false, false); a = __uint_as_float(r[0]); b = __uint_as_float(r[1]); }
DI rsrc_t mk_rsrc(const void* p, u32 bytes) { return __builtin_amdgcn_make_buffer_rsrc((void*)p, (short)0, (int)bytes, 0x00020000); }
DI u64 mix(u64 e) { return e * 0x9E3779B97F4A7C15ull + 0x632BE59BD9B4E019ull; }

template <int OFF> DI void ld128_sc1(u32x4& d, u32 voff, rsrc_t rs, u32 soff) {
    asm volatile("buffer_load_dwordx4 %0, %1, %2, %3 offen offset:%4 sc1" : "=v"(d) : "v"(voff), "s"(rs), "s"(soff), "i"(OFF));
}
template <int OFF> DI void rld128_sc1(u32x4& d, u32 voff, rsrc_t rs, u32 soff) {
    asm volatile("buffer_load_dwordx4 %0, %1, %2, %3 offen offset:%4 sc1" : "+v"(d) : "v"(voff), "s"(rs), "s"(soff), "i"(OFF));
}
DI void ld64_sc1(u32x2& d, u32 voff, rsrc_t rs, u32 soff) {
    asm volatile("buffer_load_dwordx2 %0, %1, %2, %3 offen sc1" : "=v"(d) : "v"(voff), "s"(rs), "s"(soff));
}
template <int OFF> DI void st128_sc1(u32x4 v, u32 voff, rsrc_t rs, u32 soff) {
    asm volatile("buffer_store_dwordx4 %0, %1, %2, %3 offen offset:%4 sc1\n\ts_nop 1" :: "v"(v), "v"(voff), "s"(rs), "s"(soff), "i"(OFF) : "memory");
}
template <int OFF> DI void st64_sc1(u32x2 v, u32 voff, rsrc_t rs, u32 soff) {
    asm volatile("buffer_store_dwordx2 %0, %1, %2, %3 offen offset:%4 sc1" :: "v"(v), "v"(voff), "s"(rs), "s"(soff), "i"(OFF) : "memory");
}
template <int LDSOFF> DI void set_m0(u32 base) { asm volatile("s_add_u32 m0, %0, %1" :: "s"(base), "i"(LDSOFF) : "scc"); }
DI void glds_go(u32 voff, rsrc_t rs, u32 soff) {
    asm volatile("buffer_load_dwordx4 %0, %1, %2 offen lds" :: "v"(voff), "s"(rs), "s"(soff)); }
template <int LDSOFF> DI void glds(u32 base, u32 voff, rsrc_t rs, u32 soff) {
    asm volatile("s_add_u32 m0, %0, %4\n\ts_nop 0\n\tbuffer_load_dwordx4 %1, %2, %3 offen lds"
                 :: "s"(base), "v"(voff), "s"(rs), "s"(soff), "i"(LDSOFF) : "scc");
}
template <int N> DI void wait_vm() { asm volatile("s_waitcnt vmcnt(%0)" :: "i"(N) : "memory"); }
DI void pin(u32x4& a) { asm volatile("" : "+v"(a)); }
DI void pin(u32x2& a) { asm volatile("" : "+v"(a)); }
DI void pin(u32& a) { asm volatile("" : "+v"(a)); }
#define PINE(v, i) do { u32 e_ = (v)[i]; pin(e_); (v)[i] = e_; } while (0)
DI bool all_eq(u32x2 v, u32x2 tag) { return __builtin_amdgcn_ballot_w64((v[0] != tag[0]) | (v[1] != tag[1])) == 0; }

DI void poll(rsrc_t ws, u32 voff, u32x2 tag) {
#pragma unroll 1
    for (int it = 0; it < (1 << 18); ++it) {
        u32x2 v;
        asm volatile("buffer_load_dwordx2 %0, %1, %2, 0 offen sc1\n\ts_waitcnt vmcnt(0)" : "=v"(v) : "v"(voff), "s"(ws) : "memory");
        if (all_eq(v, tag)) break;
        __builtin_amdgcn_s_sleep(1);
    }
}

#define TS(i) do {} while (0)

constexpr float LOG2E = 1.4426950408889634f;
DI u32 phi2(float a, float b) {
    f32x2 u = {a, b};
    f32x2 y = __builtin_elementwise_fma(u, (f32x2){LOG2E, LOG2E}, (f32x2){-LOG2E, -LOG2E});
    f32x2 r = {vmax(u[0], __builtin_amdgcn_fmed3f(__builtin_amdgcn_exp2f(y[0]), 0.f, 1.f)),
               vmax(u[1], __builtin_amdgcn_fmed3f(__builtin_amdgcn_exp2f(y[1]), 0.f, 1.f))};
    r = r + 1e-6f;
    return pk2(r[0], r[1]);
}
DI void pinf(float& a) { asm volatile("" : "+v"(a)); }

template <bool ISA>
DI void body(const u16* __restrict__ xg, const u16* __restrict__ wg_, const u16* __restrict__ wog, u16* __restrict__ outg,
             char* wsp, int t, int g, char* smem) {
    constexpr const Sched& SC = ISA ? SCHED_A : SCHED_B;
    constexpr int M_FLAG = m_flag(ISA), M_POLL = m_poll(ISA), M_CHK = m_chk(ISA);
    const int tid = threadIdx.x;
    const int lane = tid & 63;
    const int w = __builtin_amdgcn_readfirstlane(tid >> 6);
    const int gam = lane >> 4, c16 = lane & 15, hc = c16 >> 2;
    const int b = ISA ? 0 : 1;
    const int wg = (b * 16 + t) * 8 + g;
    const u32 lds0 = (u32)(size_t)(LAS char*)smem;
    TS(0);

    u32 cpf;
    {
        u64 pc; u32 len;
        if constexpr (ISA) asm volatile("s_getpc_b64 %0\n.Lfk_pc_a:\n\ts_mov_b32 %1, .Lfk_end_a-.Lfk_pc_a" : "=s"(pc), "=s"(len));
        else               asm volatile("s_getpc_b64 %0\n.Lfk_pc_b:\n\ts_mov_b32 %1, .Lfk_end_b-.Lfk_pc_b" : "=s"(pc), "=s"(len));
        len = len > 65536u ? 0u : len;
        const rsrc_t rsC = mk_rsrc((const void*)pc, len);
        asm volatile("buffer_load_dword %0, %1, %2, 0 offen" : "=v"(cpf) : "v"(lane < 16 ? (u32)((g & 3) + 4 * (lane + 16 * w)) * 128u : OOB), "s"(rsC));
    }
    SB();
    const rsrc_t rsW = mk_rsrc(wg_, DM * 3 * DM * 2);
    const rsrc_t rsX = mk_rsrc(xg + (size_t)(t * 128 + w * 32) * DM, 32 * DM * 2);
    const int rot = (t + g) % 5;
    u32 rck[5];
#pragma unroll
    for (int j = 0; j < 5; ++j) { int c = j + rot; c = c >= 5 ? c - 5 : c; rck[j] = (u32)c; }
    const u32 ldsw = lds0 + (u32)w * 5120u;
    const u32 ldsx = lds0 + XST_OFF + (u32)w * 20480u;
    u32 vx[4];
#pragma unroll
    for (int j = 0; j < 4; ++j) { int i = 8 * (j & 1) + (lane >> 3); vx[j] = (u32)((2 * i + (j >> 1)) * 1280 + (((lane & 7) ^ (i >> 1)) * 16)); }
    auto soffX = [&](auto H) { return rck[H.v >> 1] * 256u + (u32)(H.v & 1) * 128u; };
    auto issueX = [&](auto H) { const u32 so = soffX(H); sfor<0, 4>([&](auto J) { glds<(H.v % 5) * 4096 + J.v * 1024>(ldsx, vx[J.v], rsX, so); }); };
    issueX(IC<0>{});
    SB();
    u32 vw[5];
#pragma unroll
    for (int i = 0; i < 5; ++i) {
        int c = i * 64 + lane;
        int rl = c / 10, ch = c - rl * 10;
        vw[i] = (u32)((rl ^ ((rl >> 1) & 4)) * 3840 + ch * 16);
    }
    const u32 wbase = (u32)w * (32u * 3840u) + (u32)g * 160u;
    auto soffW = [&](auto S) { return rck[st_chunk(S.v)] * (128u * 3840u) + (u32)(st_type(S.v) * 1280) + wbase; };
    auto issueW = [&](auto S) { const u32 so = soffW(S); sfor<0, 5>([&](auto I) { glds<st_slot(S.v) * SLOT_B + I.v * 1024>(ldsw, vw[I.v], rsW, so); }); };
    issueW(IC<0>{});
    const rsrc_t rsWo = mk_rsrc(wog, DM * DM * 2);
    const rsrc_t rsWS = mk_rsrc(wsp, WS_BYTES);
    u16* const outp = outg + (size_t)(t * 128 + w * 32) * (2 * DM) + b * DM + g * 80;
    const rsrc_t rsO = mk_rsrc(outp, 31 * 2 * DM * 2 + 160);
    u32x2 ep; u32 touch;
    {
        const u32 tv = lane == 0 ? (ISA ? (u32)(128 * (1 + t)) : 0u) : lane == 1 ? (ISA ? (u32)(128 * t) : 0u)
                     : lane == 2 ? WS_O + (u32)((b * 16 + t) * 4 + w) * OBLK + 40960u : lane == 3 ? WS_P1 + (u32)wg * 8192u + 8064u : lane == 4 ? WS_O - 128u : OOB;
        ld64_sc1(ep, tv, rsWS, 0u);
        const rsrc_t rsT = mk_rsrc((w & 1) ? (const void*)wog : (const void*)outp, (w & 1) ? DM * DM * 2 : 160);
        asm volatile("buffer_load_dword %0, %1, %2, 0 offen" : "=v"(touch) : "v"((u32)lane * 65536u), "s"(rsT));
    }
    SB();

    u32 vo[5], vo4[5];
#pragma unroll
    for (int i = 0; i < 5; ++i) {
        int c = i * 64 + lane;
        int rl = c / 10, ch = c - rl * 10;
        int k = rl ^ ((rl >> 1) & 4);
        int ga = k >> 3, e = k & 7;
        vo[i] = (u32)((20 * (e >> 2) + 4 * ga + (e & 3)) * 1280 + ch * 16);
        vo4[i] = (u32)((80 * ((g + 2 * w + (e >> 2)) & 7) + 20 * ga + 16 + (e & 3)) * 1280 + ch * 16);
        asm volatile("" : "+v"(vo[i]), "+v"(vo4[i]));
    }
    auto soffO = [&](auto S) {
        constexpr int ck = st_chunk(S.v);
        const u32 row = ck < 4 ? 80u * (u32)((g + 2 * ck + (w >> 1)) & 7) + 40u * (u32)(w & 1) : 0u;
        return row * 1280u + (u32)g * 160u;
    };

    u32 rb[2][2][2];
    {
        int q = (lane >> 2) & 3, p = lane & 3;
#pragma unroll
        for (int tt = 0; tt < 2; ++tt) {
            int row = 8 * gam + 4 * (tt ^ (gam & 1)) + q;
            rb[0][tt][0] = lds0 + (u32)(row * 160 + 8 * p);
            rb[0][tt][1] = lds0 + (u32)(row * 160 + 40 * p + 32);
            rb[1][tt][0] = rb[0][tt][0] + 3 * SLOT_B;
            rb[1][tt][1] = rb[0][tt][1] + 3 * SLOT_B;
            asm volatile("" : "+v"(rb[0][tt][0]), "+v"(rb[0][tt][1]), "+v"(rb[1][tt][0]), "+v"(rb[1][tt][1]));
        }
    }
    u32x2 Bh[2][5][2];
    auto readB = [&](auto N, auto F, auto T) {
        constexpr int n = N.v, s = n >> 2, kk = n & 3, slot = st_slot(s), hf = slot / 3, f = F.v;
        constexpr int off = (slot % 3) * SLOT_B + kk * 5120;
        Bh[n & 1][f][T.v] = f < 4 ? trread(rb[hf][T.v][0] + off + 40 * f) : trread(rb[hf][T.v][1] + off);
    };
    auto readBall = [&](auto N) { sfor<0, 5>([&](auto F) { readB(N, F, IC<0>{}); readB(N, F, IC<1>{}); SB(); }); };
    u32 rx[2];
#pragma unroll
    for (int kk = 0; kk < 2; ++kk) { rx[kk] = ldsx + (u32)(c16 * 128 + (((4 * kk + gam) ^ (c16 >> 1)) * 16)); asm volatile("" : "+v"(rx[kk])); }
    u32x4 xf[2][20];
    auto readX = [&](auto H, auto J) {
        constexpr int h = H.v, p = J.v & 1, kk = J.v >> 1;
        xf[p][h * 2 + kk] = lds_ld<u32x4>(rx[kk] + (h % 5) * 4096 + p * 2048);
    };

    f32x4 acc[2][5], acv[2][5];
    const u32 xch = lds0 + XCH_OFF;
    u32 vl = (u32)lane * 16u;
    const u32 myP1 = WS_P1 + (u32)wg * 8192u;
    const u32 obase = WS_O + (u32)((b * 16 + t) * 4 + w) * OBLK;
    u32 vflag = lane < 8 ? vl : OOB;
    u32 vh0 = (ISA && w == 3 && lane >= 32) ? OOB : vl;
    u32 vh1 = (ISA && w == 3) ? OOB : vl;
    u32 f1src = ISA ? WS_F1 + (u32)((t > 0 ? wg - 8 : wg) * 4 + w) * 128u : WS_F1 + (u32)((128 + g) * 4 + w) * 128u + (u32)c16 * 4096u;
    u32 f2src = WS_F2 + (u32)(((b * 16 + t) * 8 + (lane & 7)) * 4 + w) * 128u;
    u32 vst0 = xch + (u32)(w * 5120 + c16 * 320 + gam * 8), vst1 = xch + (u32)(w * 5120 + c16 * 320 + gam * 40 + 32);
    u32 vout[5];
#pragma unroll
    for (int i = 0; i < 5; ++i) { int c = i * 64 + lane; int rl = c / 10; vout[i] = (u32)(rl * 2560 + (c - rl * 10) * 16); asm volatile("" : "+v"(vout[i])); }
    u32x4 on = {0x3F803F80u, 0x3F803F80u, 0x3F803F80u, 0x3F803F80u};
    asm volatile("" : "+v"(vl), "+v"(vflag), "+v"(vh0), "+v"(vh1), "+v"(f1src), "+v"(f2src), "+v"(vst0), "+v"(vst1), "+v"(on));
    u32x2 tag, tagp; u32x4 tag4;
    u32 wpf, vwpf = lane < 50 ? (u32)((((t & 3) * 8 + g) * 4 + w) * 50 + lane) * 128u : OOB;
    asm volatile("" : "+v"(vwpf));

    f32x4 cown, cprv; u32x2 mown, mprv;
    u32 kwr, vwr, krd, vrd;
    u32x4 S1;
    u32 a1m[4], a2m[4], a2n[4], a3m[4], l8, zrd, vgl, swr;
    if constexpr (ISA) {
        const bool hasprev = (w > 0) || (t > 0);
        bool vo_[4], vp_[4];
#pragma unroll
        for (int j = 0; j < 4; ++j) {
            int kk = 4 * gam + j;
            vo_[j] = (kk <= c16) && (kk >= c16 - 7);
            vp_[j] = (kk >= c16 + 9) && hasprev;
            cown[j] = vo_[j] ? 0.f : -1e30f;
            cprv[j] = vp_[j] ? 0.f : -1e30f;
        }
        mown[0] = (vo_[0] ? 0xFFFFu : 0u) | (vo_[1] ? 0xFFFF0000u : 0u);
        mown[1] = (vo_[2] ? 0xFFFFu : 0u) | (vo_[3] ? 0xFFFF0000u : 0u);
        mprv[0] = (vp_[0] ? 0xFFFFu : 0u) | (vp_[1] ? 0xFFFF0000u : 0u);
        mprv[1] = (vp_[2] ? 0xFFFFu : 0u) | (vp_[3] ? 0xFFFF0000u : 0u);
        kwr = xch + (c16 >= 8 ? (u32)(w + 1) * 8192u : 0u) + (u32)(gam * 8 + (c16 & 7)) * 16u;
        vwr = xch + (gam >= 2 ? (u32)(w + 1) * 8192u : 0u) + 4096u + (u32)(lane & 31) * 8u;
        krd = xch + (u32)w * 8192u + (u32)(gam * 8 + (c16 & 7)) * 16u;
        vrd = xch + (u32)w * 8192u + 4096u + (u32)(lane & 31) * 8u;
        asm volatile("" : "+v"(cown), "+v"(cprv), "+v"(mown), "+v"(mprv), "+v"(kwr), "+v"(vwr), "+v"(krd), "+v"(vrd));
    } else {
        const u32 one = ((gam & 1) == (c16 >> 3)) ? ((c16 & 1) ? 0x3F800000u : 0x00003F80u) : 0u;
#pragma unroll
        for (int d = 0; d < 4; ++d) S1[d] = ((c16 >> 1) & 3) == d ? one : 0u;
        vgl = (u32)(lane & 31) * 16u + (u32)(lane >> 5) * (8u * 8u * 8192u);
        swr = xch + 32768u + (u32)(2 * w + (gam & 1)) * 512u + (u32)((gam >> 1) * 16 + c16) * 8u;
        l8 = xch + 32768u + (u32)lane * 8u; zrd = xch + 32768u + (u32)(6 * 512) + (u32)gam * 128u;
        const u32 zb = xch + 32768u + 4096u;
#pragma unroll
        for (int h = 0; h < 4; ++h) {
            a1m[h] = gam == h ? l8 + 4 * 512 : zb;
            a2m[h] = hc == h ? l8 + 5 * 512 : zb;
            a2n[h] = (hc == h && gam == h) ? l8 + 7 * 512 : zb;
            a3m[h] = gam == h ? xch + 32768u + (u32)(7 * 512 + h * 128 + ((h + 1) & 3) * 32) : zb;
            asm volatile("" : "+v"(a1m[h]), "+v"(a2m[h]), "+v"(a2n[h]), "+v"(a3m[h]));
        }
        asm volatile("" : "+v"(S1), "+v"(l8), "+v"(zrd), "+v"(vgl), "+v"(swr));
    }

    u32x4 Kown[2][4], Kprv[2][4], Vop[2][5];
    u32x4 PK[5], VV[5], pub;
    f32x4 G[4], d1[4], d2[4], zz[4], d3, z4, rs4[2][4];
    float ph[2];
    u32 gwr = xch + (u32)w * 8192u + vl, grd = xch + (u32)w * 2048u + vl;
    asm volatile("" : "+v"(gwr), "+v"(grd));
    u32x4 hcp[2];
    u32x2 flg, fl2;
    u32x4 gl[16];

    auto filler = [&](auto M) {
        constexpr int m = M.v;
        if constexpr (m == 60) {
            pin(ep); pin(touch); pin(cpf);
            u64 a = mix(((u64)(u32)__builtin_amdgcn_readlane(ep[1], 0) << 32) | (u32)__builtin_amdgcn_readlane(ep[0], 0));
            u64 c = mix(((u64)(u32)__builtin_amdgcn_readlane(ep[1], 1) << 32) | (u32)__builtin_amdgcn_readlane(ep[0], 1));
            tag = (u32x2){(u32)a, (u32)(a >> 32)}; tagp = (u32x2){(u32)c, (u32)(c >> 32)};
            tag4 = cat(tag, tag); pin(tag4);
            asm volatile("" : "+s"(tagp));
        }
        if constexpr (m == M_WPF) asm volatile("buffer_load_dword %0, %1, %2, 0 offen" : "=v"(wpf) : "v"(vwpf), "s"(rsWo));
        if constexpr (ISA) {
            if constexpr (m >= 142 && m < 182) {
                constexpr int i = (m - 142) / 5, d = (m - 142) % 5, p = i >> 2, h = i & 3, f = d < 2 ? h : 4;
                if constexpr (d < 4) { u32 v_ = pk2(acc[p][f][(d & 1) * 2], acc[p][f][(d & 1) * 2 + 1]); pin(v_); Kown[p][h][d] = v_; if constexpr (d == 3) asm volatile("" : "+v"(acc[p][4])); }
                else lds_st(kwr + i * 512, Kown[p][h]);
            }
            if constexpr (m == M_PUBA) {
                hcp[0] = lds_ld<u32x4>(xch + 4 * 8192 + (u32)w * 2048u + vl);
                hcp[1] = lds_ld<u32x4>(xch + 4 * 8192 + (u32)w * 2048u + vl + 1024);
            }
            if constexpr (m == M_PUBA + 4) st128_sc1<0>(hcp[0], vh0, rsWS, myP1 + (u32)w * 2048u);
            if constexpr (m == M_PUBA + 5) st128_sc1<1024>(hcp[1], vh1, rsWS, myP1 + (u32)w * 2048u);
            if constexpr (m >= 295) {
                constexpr int i = m - 295;
                if constexpr (i < 4) { Kprv[i >> 1][(i & 1) * 2] = lds_ld<u32x4>(krd + 2 * i * 512); Kprv[i >> 1][(i & 1) * 2 + 1] = lds_ld<u32x4>(krd + (2 * i + 1) * 512); }
#pragma unroll
                for (int q = 2 * i; q < 2 * i + 2; ++q) { u32x2 v = lds_ld<u32x2>(vrd + q * 256); Vop[q / 5][q % 5][2] = v[0]; Vop[q / 5][q % 5][3] = v[1]; }
            }
        } else {
            if constexpr (m >= 100 && m < 172) {
                constexpr int i = m - 100, pr = i / 9, st = i % 9, f = 3 + pr / 4, d = pr % 4, e = st & 1;
                const float u = acc[d >> 1][f][(d & 1) * 2 + e];
                if constexpr (st < 2) { ph[e] = __builtin_fmaf(u, LOG2E, -LOG2E); pinf(ph[e]); }
                else if constexpr (st < 4) { ph[e] = __builtin_amdgcn_fmed3f(__builtin_amdgcn_exp2f(ph[e]), 0.f, 1.f); pinf(ph[e]); }
                else if constexpr (st < 6) { ph[e] = vmax(u, ph[e]); pinf(ph[e]); }
                else if constexpr (st < 8) { ph[e] = ph[e] + 1e-6f; pinf(ph[e]); }
                else { u32 v_ = pk2(ph[0], ph[1]); pin(v_); PK[f][d] = v_; }
            }
            if constexpr (m >= 174 && m < 178) mv0(zz[m - 174], PK[m - 174], on);
            if constexpr (m == 178) mv0(z4, PK[4], on);
            if constexpr (m >= 181 && m < 193) {
                constexpr int j = (m - 181) / 3, h = 1 + (m - 181) % 3;
                float v_ = hc == h ? zz[h][j] : zz[0][j]; pinf(v_); zz[0][j] = v_;
            }
            if constexpr (m == 193) lds_st(gwr + 6 * 1024, zz[0]);
            if constexpr (m == 194) lds_st(l8 + 4096u, (u32x2){0u, 0u});
            if constexpr (m >= 200 && m < 205) {
                constexpr int f = m == 200 ? 0 : m == 201 ? 1 : m == 202 ? 4 : m == 203 ? 2 : 3;
                VV[f] = cat(pk4(acv[0][f]), pk4(acv[1][f])); pin(VV[f]);
            }
            if constexpr (m == 202) { mv0(d1[0], PK[4], VV[0]); mv0(G[0], PK[0], VV[0]); }
            if constexpr (m == 203) { mv0(d1[1], PK[4], VV[1]); mv0(G[1], PK[1], VV[1]); }
            if constexpr (m == 204) { mv0(d2[0], PK[0], VV[4]); mv0(d2[1], PK[1], VV[4]); }
            if constexpr (m == 205) { mv0(d2[2], PK[2], VV[4]); mv0(d2[3], PK[3], VV[4]); }
            if constexpr (m == 206) { mv0(d3, PK[4], VV[4]); mv0(d1[2], PK[4], VV[2]); }
            if constexpr (m == 207) { mv0(d1[3], PK[4], VV[3]); mv0(G[2], PK[2], VV[2]); }
            if constexpr (m == 208) mv0(G[3], PK[3], VV[3]);
            if constexpr (m == 205 || m == 206) lds_st(gwr + (m - 205) * 1024, G[m - 205]);
            if constexpr (m >= 208 && m < 211) {
#pragma unroll
                for (int j = 0; j < 4; ++j) { float v_ = hc == m - 207 ? d2[m - 207][j] : d2[0][j]; pinf(v_); d2[0][j] = v_; }
            }
            if constexpr (m == 209) {
#pragma unroll
                for (int j = 0; j < 4; ++j) { float v_ = gam == hc ? d3[j] : z4[j]; pinf(v_); d3[j] = v_; }
            }
            if constexpr (m == 210 || m == 211) lds_st(gwr + (m - 208) * 1024, G[m - 208]);
            if constexpr (m >= 211 && m < 214) {
#pragma unroll
                for (int j = 0; j < 4; ++j) { float v_ = gam == m - 210 ? d1[m - 210][j] : d1[0][j]; pinf(v_); d1[0][j] = v_; }
            }
            if constexpr (m == 212) lds_st(gwr + 5 * 1024, d2[0]);
            if constexpr (m == 213) lds_st(gwr + 7 * 1024, d3);
            if constexpr (m == 214) lds_st(gwr + 4 * 1024, d1[0]);
            if constexpr (m == 215 || m == 216) {
#pragma unroll
                for (int q = 0; q < 4; ++q) rs4[m - 215][q] = lds_ld<f32x4>(grd + (m - 215) * 1024 + q * 8192);
            }
            if constexpr (m >= 219 && m < 223) {
                constexpr int i = (m - 219) >> 1, j0 = ((m - 219) & 1) * 2;
                float v0 = rs4[i][0][j0] + rs4[i][1][j0]; pinf(v0); float v1 = rs4[i][0][j0 + 1] + rs4[i][1][j0 + 1]; pinf(v1);
                float v2 = rs4[i][2][j0] + rs4[i][3][j0]; pinf(v2); float v3 = rs4[i][2][j0 + 1] + rs4[i][3][j0 + 1]; pinf(v3);
                v0 += v2; pinf(v0); v1 += v3; pinf(v1);
                u32 v_ = pk2(v0, v1); pin(v_); pub[m - 219] = v_;
            }
            if constexpr (m == M_PUBB) st128_sc1<0>(pub, vl, rsWS, myP1 + (u32)w * 1024u);
            if constexpr (m >= M_CHK && m < M_CHK + 16) ld128_sc1<((m - M_CHK) & 1) * 512>(gl[m - M_CHK], vgl, rsWS, WS_P1 + (u32)((16 + ((((m - M_CHK) >> 1) + 2 * t) & 7)) * 8 + g) * 8192u + (u32)w * 1024u);
        }
        if constexpr (m == M_POLL - 1) ld64_sc1(fl2, f2src, rsWS, 0u);
        if constexpr (m == M_POLL) ld64_sc1(flg, f1src, rsWS, 0u);
    };

    auto sync = [&](auto S, auto LG) {
        constexpr int s = S.v;
        static_assert(SC.n_sync[s] >= 0, "W piece not issued yet");
        constexpr int xc = sync_mb(ISA, s) % 20 == 15 ? xrd_half(sync_mb(ISA, s) / 5) : -1;
        constexpr int nw = (xc >= 0 && SC.n_x[xc < 0 ? 0 : xc] < SC.n_sync[s]) ? SC.n_x[xc < 0 ? 0 : xc] : SC.n_sync[s];
        SB();
        if constexpr (LG.v) asm volatile("s_waitcnt vmcnt(%0) lgkmcnt(0)\n\ts_barrier" :: "i"(cap63(nw)) : "memory");
        else asm volatile("s_waitcnt vmcnt(%0)\n\ts_barrier" :: "i"(cap63(nw)) : "memory");
        SB();
    };
    auto group = [&](auto M) {
        constexpr int m = M.v, n = m / 5, f = m % 5, s = n >> 2, kk = n & 3, ty = st_type(s), ck = st_chunk(s);
        constexpr bool trans = ty == 0 || (ISA && ty == 1);
        constexpr int first = (ck == 0 && kk == 0) ? ((!ISA && ty != 2) ? 2 : 1) : 0;
        constexpr bool pre = (n + 1) % 20 != 0 || n == (ISA ? 19 : 39);
        constexpr int xc = xrd_half(n);
        static_assert(xc < 0 || SC.n_x[xc < 0 ? 0 : xc] >= 0, "x piece not issued yet");
        if constexpr (m == sync_mb(ISA, s)) sync(IC<s>{}, IC<(ISA ? s == 14 : s == 10)>{});
        else if constexpr (f == 0 && xc >= 0) { wait_vm<cap63(SC.n_x[xc < 0 ? 0 : xc])>(); SB(); }
        constexpr int vk = SC.kind[m], vi = SC.id[m], vp = SC.piece[m];
        const u32x4 Bw = cat(Bh[n & 1][f][0], Bh[n & 1][f][1]);
        if constexpr (vk == 1) set_m0<st_slot(vi) * SLOT_B + vp * 1024>(ldsw);
        if constexpr (vk == 2) set_m0<(vi % 5) * 4096 + vp * 1024>(ldsx);
        mml<trans, first>(ty == 2 ? acv[0][f] : acc[0][f], xf[0][ck * 4 + kk], Bw);
        SB();
        if constexpr (pre) readB(IC<n + 1>{}, IC<f>{}, IC<0>{});
        if constexpr (vk == 1) {
            if constexpr (st_type(vi) == 3) glds_go(st_chunk(vi) < 4 ? vo[vp] : vo4[vp], rsWo, soffO(IC<vi>{}));
            else glds_go(vw[vp], rsW, soffW(IC<vi>{}));
        }
        if constexpr (vk == 2) glds_go(vx[vp], rsX, soffX(IC<vi>{}));
        if constexpr (xc >= 0 && f < 4) readX(IC<xc>{}, IC<f>{});
        filler(M);
        SB();
        mml<trans, first>(ty == 2 ? acv[1][f] : acc[1][f], xf[1][ck * 4 + kk], Bw);
        SB();
        if constexpr (pre) readB(IC<n + 1>{}, IC<f>{}, IC<1>{});
        SB();
    };

    SB();
    issueX(IC<1>{}); issueW(IC<1>{}); issueX(IC<2>{});
    SB();
    wait_vm<SC.n_init>();
    sfor<0, 4>([&](auto J) { readX(IC<0>{}, J); });
    asm volatile("s_barrier" ::: "memory");
    TS(1);
    readBall(IC<0>{});

    sfor<0, 100>(group);
    if constexpr (!ISA) {
        POST_MFMA();
        f32x4 ex[6];
#pragma unroll
        for (int i = 0; i < 6; ++i) {
            const f32x4 u = acc[i & 1][i >> 1];
            const f32x2 c2 = {LOG2E, LOG2E}, m2 = {-LOG2E, -LOG2E};
            f32x2 e0 = __builtin_elementwise_fma((f32x2){u[0], u[1]}, c2, m2), e1 = __builtin_elementwise_fma((f32x2){u[2], u[3]}, c2, m2);
            ex[i] = (f32x4){e0[0], e0[1], e1[0], e1[1]};
        }
        SB();
#pragma unroll
        for (int i = 0; i < 24; ++i) ex[i >> 2][i & 3] = __builtin_amdgcn_fmed3f(__builtin_amdgcn_exp2f(ex[i >> 2][i & 3]), 0.f, 1.f);
        SB();
#pragma unroll
        for (int i = 0; i < 24; ++i) ex[i >> 2][i & 3] = vmax(acc[(i >> 2) & 1][i >> 3][i & 3], ex[i >> 2][i & 3]);
        SB();
#pragma unroll
        for (int i = 0; i < 6; ++i) ex[i] = ex[i] + 1e-6f;
        SB();
#pragma unroll
        for (int f = 0; f < 3; ++f) { PK[f] = cat(pk4(ex[2 * f]), pk4(ex[2 * f + 1])); pin(PK[f]); }
    }
    if constexpr (!ISA) { sync(IC<4>{}, IC<0>{}); readBall(IC<20>{}); }
    sfor<100, 200>(group);
    TS(2);
    if constexpr (ISA) {
        POST_MFMA();
#pragma unroll
        for (int i = 0; i < 10; ++i) {
            u32x2 v = pk4(acv[i / 5][i % 5]);
            Vop[i / 5][i % 5][0] = v[0]; Vop[i / 5][i % 5][1] = v[1];
            lds_st(vwr + i * 256, v);
        }
        sync(IC<9>{}, IC<1>{});
        readBall(IC<40>{});
    }
    TS(3);

    sfor<200, M_FLAG>(group);
    wait_vm<SC.pos[M_FLAG] - 1 - SC.pos[ISA ? M_PUBA + 6 : M_PUBB + 1]>();
    st128_sc1<0>(tag4, vflag, rsWS, WS_F1 + (u32)(wg * 4 + w) * 128u);
    SB();
    sfor<M_FLAG, M_CHK>(group);
    TS(4);
    {
        wait_vm<SC.pos[M_CHK] - xpre(ISA, M_CHK) - SC.pos[M_POLL + 1]>();
        pin(flg); pin(fl2); pin(wpf);
        if constexpr (ISA) {
            if (t > 0 && !all_eq(flg, tagp)) poll(rsWS, f1src, tagp);
            SB();
            const u32 so = t > 0 ? myP1 - 8u * 8192u + (u32)w * 2048u : OOB;
            ld128_sc1<0>(hcp[0], vh0, rsWS, so);
            ld128_sc1<1024>(hcp[1], vh1, rsWS, so);
        } else {
            if (!all_eq(flg, tag)) poll(rsWS, f1src, tag);
        }
        SB();
    }
    TS(5);
    sfor<M_CHK, M_PARK>(group);
    if constexpr (ISA) {
        wait_vm<SC.pos[M_PARK] - SC.pos[M_CHK]>();
        pin(hcp[0]); pin(hcp[1]);
        lds_st(xch + (u32)w * 2048u + vl, hcp[0]);
        lds_st(xch + (u32)w * 2048u + vl + 1024, hcp[1]);
        SB();
    }
    sfor<M_PARK, 300>(group);
    auto wopiece = [&](auto J) {
        constexpr int st = 15 + NOIN + J.v / 5, i = J.v % 5;
        if constexpr (st < 20) { SB(); glds<st_slot(st) * SLOT_B + i * 1024>(ldsw, st_chunk(st) < 4 ? vo[i] : vo4[i], rsWo, soffO(IC<st>{})); SB(); }
    };
    static_assert(NOIN >= 3, "at most ten pieces are placed in the epilogue");
    POST_MFMA();
    TS(6);

    u32x4 of[2][20];
    u32x4 hq[8];
    auto publish = [&](auto P, u32x2 h4) {
        constexpr int p = P.v;
        st128_sc1<0>(of[p][0], vl, rsWS, obase + (u32)p * 16384u + (u32)g * 2048u);
        st128_sc1<1024>(of[p][1], vl, rsWS, obase + (u32)p * 16384u + (u32)g * 2048u);
        hq[0][2 * p] = h4[0]; hq[0][2 * p + 1] = h4[1];
        if constexpr (p == 1) st128_sc1<0>(hq[0], vl, rsWS, obase + 32768u + (u32)g * 1024u);
    };
    if constexpr (ISA) {
        u32x4 Qf[2][4];
        f32x4 so[2][4], sp[2][4];
        sfor<0, 2>([&](auto P) {
            constexpr int p = P.v;
            u32x2 q4 = pk4(acc[p][4]);
            sfor<0, 4>([&](auto H) {
                constexpr int h = H.v, i = p * 4 + h;
                Qf[p][h] = cat(pk4(acc[p][h]), (u32x2){gam == h ? q4[0] : 0u, gam == h ? q4[1] : 0u});
                SB();
                if constexpr (i % 2 == 0) wopiece(IC<i / 2>{});
                if constexpr (i > 0) {
                    mvc(so[(i - 1) >> 2][(i - 1) & 3], Kown[(i - 1) >> 2][(i - 1) & 3], Qf[(i - 1) >> 2][(i - 1) & 3], cown);
                    mvc(sp[(i - 1) >> 2][(i - 1) & 3], Kprv[(i - 1) >> 2][(i - 1) & 3], Qf[(i - 1) >> 2][(i - 1) & 3], cprv);
                    SB();
                }
            });
        });
        mvc(so[1][3], Kown[1][3], Qf[1][3], cown);
        mvc(sp[1][3], Kprv[1][3], Qf[1][3], cprv);
        SB();
        asm volatile("s_nop 2" :: "v"(cown), "v"(cprv));
        SB();
        const float cs = 0.22360679774997896f * 1.4426950408889634f;
        f32x4 oh[2][4], o4[2][4], dn[2][4];
        u32x4 Pf[2][4];
        auto pv = [&](auto P, auto I) {
            constexpr int p = P.v, h = I.v / 3, k = I.v % 3;
            SB();
            if constexpr (k == 0) mv0(dn[p][h], on, Pf[p][h]); else if constexpr (k == 1) mv0(oh[p][h], Vop[p][h], Pf[p][h]); else mv0(o4[p][h], Vop[p][4], Pf[p][h]);
            SB();
        };
        sfor<0, 2>([&](auto P) {
            constexpr int p = P.v;
            f32x4 L[4]; float mx[4];
            sfor<0, 4>([&](auto H) {
                constexpr int h = H.v;
#pragma unroll
                for (int j = 0; j < 4; ++j) L[h][j] = vmax(so[p][h][j], sp[p][h][j]);
                mx[h] = vmax(vmax(vmax(L[h][0], L[h][1]), L[h][2]), L[h][3]);
                if constexpr (p == 0 && h % 2 == 0) wopiece(IC<4 + h / 2>{});
                if constexpr (p == 1) pv(IC<0>{}, IC<h>{});
            });
            swap16(mx[0], mx[1]); swap16(mx[2], mx[3]);
            float xa = vmax(mx[0], mx[1]), xb = vmax(mx[2], mx[3]);
            if constexpr (p == 1) pv(IC<0>{}, IC<4>{});
            swap32(xa, xb);
            float z0 = vmax(xa, xb) * -cs, z1 = z0;
            if constexpr (p == 1) pv(IC<0>{}, IC<5>{});
            swap32(z0, z1);
            float m0 = z0, m1 = z0, m2 = z1, m3 = z1;
            if constexpr (p == 1) pv(IC<0>{}, IC<6>{});
            swap16(m0, m1); swap16(m2, m3);
            const float mc[4] = {m0, m1, m2, m3};
            if constexpr (p == 1) pv(IC<0>{}, IC<7>{});
            sfor<0, 4>([&](auto H) {
                constexpr int h = H.v;
                u32 p0 = pk2(__builtin_amdgcn_exp2f(__builtin_fmaf(L[h][0], cs, mc[h])), __builtin_amdgcn_exp2f(__builtin_fmaf(L[h][1], cs, mc[h])));
                u32 p1 = pk2(__builtin_amdgcn_exp2f(__builtin_fmaf(L[h][2], cs, mc[h])), __builtin_amdgcn_exp2f(__builtin_fmaf(L[h][3], cs, mc[h])));
                Pf[p][h] = (u32x4){p0 & mown[0], p1 & mown[1], p0 & mprv[0], p1 & mprv[1]};
                if constexpr (p == 0) wopiece(IC<6 + h>{});
                if constexpr (p == 1) pv(IC<0>{}, IC<8 + h>{});
            });
            SB();
        });
        sfor<0, 2>([&](auto P) {
            constexpr int p = P.v;
            if constexpr (p == 1) POST_MFMA();
            float r[4]; u32x2 o2[4];
            sfor<0, 4>([&](auto H) {
                constexpr int h = H.v;
                if constexpr (p == 0) { pv(IC<1>{}, IC<2 * h>{}); }
                asm volatile("" : "+v"(dn[p][h]));
                r[h] = __builtin_amdgcn_rcpf(dn[p][h][0]);
                f32x4 t4 = {oh[p][h][0] * r[h], oh[p][h][1] * r[h], oh[p][h][2] * r[h], oh[p][h][3] * r[h]};
                if constexpr (p == 0) { pv(IC<1>{}, IC<2 * h + 1>{}); }
                o2[h] = pk4(t4);
            });
            f32x4 s4;
            sfor<0, 4>([&](auto J) {
                if constexpr (p == 0) { pv(IC<1>{}, IC<8 + J.v>{}); }
                s4[J.v] = sel4(gam, o4[p][0][J.v], o4[p][1][J.v], o4[p][2][J.v], o4[p][3][J.v]);
            });
            float r4 = sel4(gam, r[0], r[1], r[2], r[3]);
            of[p][0] = cat(o2[0], o2[1]); of[p][1] = cat(o2[2], o2[3]);
            publish(P, pk4(s4 * r4));
            SB();
        });
    } else {
        u32x4 Qf[2][4];
        f32x4 sm[2], ex[2][5];
        u32x4 A1[4], A2[4], A3[4];
        f32x4 nh[2][4], n4[2], dn[2][4];
        auto nd = [&](auto P, auto I) {
            constexpr int p = P.v, h = I.v / 3, k = I.v % 3;
            SB();
            if constexpr (k == 0) mv0(dn[p][h], A3[h], Qf[p][h]); else if constexpr (k == 1) mv0(nh[p][h], A1[h], Qf[p][h]);
            else if constexpr (h == 0) mv0(n4[p], A2[0], Qf[p][0]); else mva(n4[p], A2[h], Qf[p][h]);
            SB();
        };
        auto red = [&](auto J) {
            constexpr int j = J.v;
            SB();
            if constexpr (!(j & 3)) { wait_vm<cap63(SC.pos[NMG] - SC.pos[M_CHK + j + 4 < NMG ? M_CHK + j + 4 : NMG])>(); pin(gl[j]); pin(gl[j + 1]); pin(gl[j + 2]); pin(gl[j + 3]); SB(); }
            if constexpr (j < 2) mv0(sm[j & 1], S1, gl[j]); else mva(sm[j & 1], S1, gl[j]);
            SB();
        };
#pragma unroll
        for (int i = 0; i < 10; ++i) {
            const f32x4 u = acc[i / 5][i % 5];
            const f32x2 c2 = {LOG2E, LOG2E}, m2 = {-LOG2E, -LOG2E};
            f32x2 e0 = __builtin_elementwise_fma((f32x2){u[0], u[1]}, c2, m2), e1 = __builtin_elementwise_fma((f32x2){u[2], u[3]}, c2, m2);
            ex[i / 5][i % 5] = (f32x4){e0[0], e0[1], e1[0], e1[1]};
        }
        SB();
#pragma unroll
        for (int i = 0; i < 10; ++i)
#pragma unroll
            for (int j = 0; j < 4; ++j) ex[i / 5][i % 5][j] = __builtin_amdgcn_fmed3f(__builtin_amdgcn_exp2f(ex[i / 5][i % 5][j]), 0.f, 1.f);
        SB();
        sfor<0, 20>([&](auto I) {
            constexpr int i = I.v >> 1, j = (I.v & 1) * 2;
            if constexpr (I.v < 16) red(I);
            float v0 = vmax(acc[i / 5][i % 5][j], ex[i / 5][i % 5][j]); pinf(v0); ex[i / 5][i % 5][j] = v0;
            float v1 = vmax(acc[i / 5][i % 5][j + 1], ex[i / 5][i % 5][j + 1]); pinf(v1); ex[i / 5][i % 5][j + 1] = v1;
            SB();
        });
        lds_st(swr, pk4(sm[0]));
        lds_st(swr + 256u, pk4(sm[1]));
        SB();
#pragma unroll
        for (int i = 0; i < 10; ++i) ex[i / 5][i % 5] = ex[i / 5][i % 5] + 1e-6f;
        SB();
        asm volatile("s_waitcnt lgkmcnt(0)\n\ts_barrier" ::: "memory");
#pragma unroll
        for (int h = 0; h < 4; ++h) {
            A3[h] = cat(lds_ld<u32x2>(zrd + h * 32), lds_ld<u32x2>(a3m[h]));
            A1[h] = cat(lds_ld<u32x2>(l8 + h * 512), lds_ld<u32x2>(a1m[h]));
            A2[h] = cat(lds_ld<u32x2>(a2m[h]), lds_ld<u32x2>(a2n[h]));
            SB();
            Qf[0][h] = cat(pk4(ex[0][h]), pk4(ex[0][4])); pin(Qf[0][h]);
            asm volatile("" : "+v"(ex[0][4]));
            SB();
        }
        sfor<0, 12>([&](auto I) {
            nd(IC<0>{}, I);
            if constexpr (I.v < 8) {
                constexpr int h = I.v >> 1, hi = I.v & 1;
                u32x2 v_ = pk4(ex[1][hi ? 4 : h]); pin(v_); Qf[1][h][2 * hi] = v_[0]; Qf[1][h][2 * hi + 1] = v_[1];
                if constexpr (hi) asm volatile("" : "+v"(ex[1][4]));
            } else wopiece(IC<I.v - 8>{});
        });
        float r[2][4];
        sfor<0, 12>([&](auto I) {
            nd(IC<1>{}, I);
            if constexpr (I.v < 4) { asm volatile("" : "+v"(dn[0][I.v])); r[0][I.v] = dn[0][I.v][0] + 1e-6f; pinf(r[0][I.v]); }
            else if constexpr (I.v < 8) { r[0][I.v - 4] = __builtin_amdgcn_rcpf(r[0][I.v - 4]); pinf(r[0][I.v - 4]); }
            else wopiece(IC<I.v - 4>{});
        });
        sfor<0, 2>([&](auto P) {
            constexpr int p = P.v;
            if constexpr (p == 1) {
                sfor<8, 10>(wopiece);
#pragma unroll
                for (int h = 0; h < 4; ++h) { asm volatile("" : "+v"(dn[1][h])); r[1][h] = dn[1][h][0] + 1e-6f; }
                SB();
#pragma unroll
                for (int h = 0; h < 4; ++h) r[1][h] = __builtin_amdgcn_rcpf(r[1][h]);
                SB();
            }
            float r4 = sel4(gam, r[p][0], r[p][1], r[p][2], r[p][3]);
            SB();
#pragma unroll
            for (int h = 0; h < 4; ++h) nh[p][h] = nh[p][h] * r[p][h];
            n4[p] = n4[p] * r4;
            SB();
            of[p][0] = cat(pk4(nh[p][0]), pk4(nh[p][1])); of[p][1] = cat(pk4(nh[p][2]), pk4(nh[p][3]));
            publish(P, pk4(n4[p]));
            SB();
        });
    }
    TS(7);
    wait_vm<0>();
    st128_sc1<0>(tag4, vflag, rsWS, WS_F2 + (u32)(wg * 4 + w) * 128u);
    asm volatile("s_barrier" ::: "memory");
    TS(8);
#define OAHEAD 6
    auto oload = [&](auto K, auto P) {
        constexpr int k = K.v;
        if constexpr (k < 16) ld128_sc1<(k & 1) * 1024>(of[P.v][k], vl, rsWS, obase + (u32)P.v * 16384u + (u32)((g + k / 2) & 7) * 2048u);
        else if constexpr (k > 16 || P.v == 1) ld128_sc1<0>(hq[2 * (k - 16) + P.v], vl, rsWS, obase + 32768u + (u32)((g + 2 * (k - 16) + P.v) & 7) * 1024u);
    };
    auto kstep = [&](auto K) {
        constexpr int k = K.v, n = 60 + k;
        sfor<0, 5>([&](auto F) {
            constexpr int f = F.v;
            const u32x4 Bw = cat(Bh[n & 1][f][0], Bh[n & 1][f][1]);
            if constexpr (k == 0) mv0(acc[0][f], Bw, of[0][k]); else mva(acc[0][f], Bw, of[0][k]);
            SB();
            if constexpr (k < 19) readB(IC<n + 1>{}, F, IC<0>{});
            if constexpr (k >= 2 && k + OAHEAD < 20 && (f == 1 || f == 3)) oload(IC<k + OAHEAD>{}, IC<(f >> 1)>{});
            SB();
            if constexpr (k == 0) mv0(acc[1][f], Bw, of[1][k]); else mva(acc[1][f], Bw, of[1][k]);
            SB();
            if constexpr (k < 19) readB(IC<n + 1>{}, F, IC<1>{});
            SB();
        });
    };
    readBall(IC<60>{});
    sfor<0, 2>(kstep);
    poll(rsWS, f2src, tag);
    TS(9);
    sfor<2, 2 + OAHEAD>([&](auto K) { oload(K, IC<0>{}); oload(K, IC<1>{}); });
    SB();
    sfor<2, 20>([&](auto K) {
        constexpr int k = K.v, last = k + OAHEAD - 1 < 19 ? k + OAHEAD - 1 : 19;
        wait_vm<2 * (last - k) - ((k < 16 && last >= 16) ? 1 : 0)>();
        if constexpr (k < 16) { pin(of[0][k]); pin(of[1][k]); }
        else {
            u32x4& a = hq[2 * (k - 16)]; u32x4& c = hq[2 * (k - 16) + 1];
            pin(a); pin(c);
            of[0][k] = (u32x4){a[0], a[1], c[0], c[1]}; of[1][k] = (u32x4){a[2], a[3], c[2], c[3]};
            pin(of[0][k]); pin(of[1][k]);
            PRE_MFMA();
        }
        SB();
        kstep(K);
    });
    POST_MFMA();
    TS(10);
#pragma unroll
    for (int p = 0; p < 2; ++p) {
#pragma unroll
        for (int f = 0; f < 4; ++f) lds_st(vst0 + (p * 160 + f * 40), pk4(acc[p][f]));
        lds_st(vst1 + p * 160, pk4(acc[p][4]));
    }
#pragma unroll
    for (int i = 0; i < 5; ++i)
        __builtin_amdgcn_raw_buffer_store_b128(lds_ld<u32x4>(xch + (u32)w * 5120u + vl + i * 1024), rsO, vout[i], 0, OAUX);
    if (g == 0 && w == 0) {
        if constexpr (ISA) {
            if (t > 0) st128_sc1<0>(cat(tagp, tagp), vflag, rsWS, WS_EPOCH + (u32)(128 * t));
            if (t == 15) st128_sc1<0>(tag4, vflag, rsWS, WS_EPOCH + (u32)(128 * 16));
        } else {
            if (t == 0) st128_sc1<0>(tag4, vflag, rsWS, WS_EPOCH);
        }
    }
    TS(11);
    if constexpr (ISA) asm volatile(".Lfk_end_a:"); else asm volatile(".Lfk_end_b:");
}

__global__ __launch_bounds__(256, 1) void fused_kernel(const u16* __restrict__ xa, const u16* __restrict__ wa, const u16* __restrict__ woa,
                                                       const u16* __restrict__ xb, const u16* __restrict__ wb, const u16* __restrict__ wob,
                                                       u16* __restrict__ out, char* ws) {
    __shared__ __attribute__((aligned(16))) char smem[LDS_B];
    const int x = blockIdx.x & 7, idx = blockIdx.x >> 3;
    const int t = (x & 3) * 4 + (idx >> 3), g = idx & 7;
#define ASEL 0xF0
    const bool isa = (ASEL >> x) & 1;
    const u16* xp = isa ? xa : xb; const u16* wp = isa ? wa : wb; const u16* wop = isa ? woa : wob;
    asm volatile("" : "+s"(xp), "+s"(wp), "+s"(wop), "+s"(out), "+s"(ws));
    if (isa) body<true>(xp, wp, wop, out, ws, t, g, smem);
    else     body<false>(xp, wp, wop, out, ws, t, g, smem);
}

extern "C" void kernel_launch(void* const* d_in, const int* in_sizes, int n_in,
                              void* d_out, int out_size, void* d_ws, size_t ws_size,
                              hipStream_t stream) {
    fused_kernel<<<256, 256, 0, stream>>>((const u16*)d_in[0], (const u16*)d_in[1], (const u16*)d_in[2],
                                          (const u16*)d_in[3], (const u16*)d_in[4], (const u16*)d_in[5],
                                          (u16*)d_out, (char*)d_ws);
}
```

```cpp
#include <hip/hip_runtime.h>
#include <stdint.h>

typedef unsigned short u16;
typedef unsigned int u32;
typedef unsigned long long u64;
typedef __attribute__((ext_vector_type(4))) short s16x4;
typedef __attribute__((ext_vector_type(2))) __bf16 bf16x2;
typedef __attribute__((ext_vector_type(4))) float f32x4;
typedef __attribute__((ext_vector_type(2))) float f32x2;
typedef __attribute__((ext_vector_type(4))) u32 u32x4;
typedef __attribute__((ext_vector_type(2))) u32 u32x2;
typedef __amdgpu_buffer_rsrc_t rsrc_t;
#define LAS __attribute__((address_space(3)))
#define GAS __attribute__((address_space(1)))
#define DI __device__ __forceinline__
#define HD __host__ __device__
#define SB() __builtin_amdgcn_sched_barrier(0)

#define OAUX 2
#define XMAP 2

constexpr int DM = 640;
constexpr int SLOT_B = 20480;
constexpr int XST_OFF = 4 * SLOT_B;
constexpr int XCH_OFF = 6 * SLOT_B;
constexpr int LDS_B = 163840;
constexpr u32 OOB = 0x7FFFFF00u;

constexpr u32 WS_EPOCH = 0;
constexpr u32 WS_F1 = 4096;
constexpr u32 WS_F2 = WS_F1 + 256 * 4 * 128;
constexpr u32 WS_P1 = WS_F2 + 256 * 4 * 128;
constexpr u32 WS_O = WS_P1 + 256 * 8192;
constexpr u32 OBLK = 2 * 20480 + 128;
constexpr u32 WS_TS = WS_O + 2 * 16 * 4 * OBLK;
constexpr u32 WS_TS2 = WS_TS + 256 * 4 * 16 * 8;
constexpr u32 WS_BYTES = WS_TS2 + 256 * 4 * 64 * 4;

constexpr int NST = 20;
HD constexpr int st_type(int s)  { return s < 5 ? 1 : s < 10 ? 2 : s < 15 ? 0 : 3; }
HD constexpr int st_chunk(int s) { return s % 5; }
HD constexpr int st_slot(int s)  { return s < 8 ? s % 4 : (s - 4) % 6; }
HD constexpr int sync_mb(bool isa, int s) { return (isa ? s == 9 : s == 4) ? 20 * s + 20 : 20 * s + 15; }
HD constexpr int xrd_half(int n) { return (n & 1) && n < 19 ? (n + 1) / 2 : -1; }

constexpr int NMG = 300;
#ifndef M_WPF
#define M_WPF 141
#endif
constexpr int M_PUBA = 200;
#define MA_FLAG 228
#define MA_POLL 250
#define MA_CHK 272
#define MB_FLAG 246
#define MB_POLL 264
#define MB_CHK 284
constexpr int M_PUBB = 224;
HD constexpr int m_flag(bool isa) { return isa ? MA_FLAG : MB_FLAG; }
HD constexpr int m_poll(bool isa) { return isa ? MA_POLL : MB_POLL; }
HD constexpr int m_chk(bool isa) { return isa ? MA_CHK : MB_CHK; }
constexpr int M_PARK = 293;
#define LOOKA 50
#define OSPACE 3
#define OSPACEB 2
#define RI 6
#define SPV 2
#define SPQ0 2
#ifndef M_Q1
#define M_Q1 180
#endif
#define SPQ 3
#define NOIN 4
struct Sched {
    int kind[NMG], id[NMG], piece[NMG];
    int pos[NMG + 1];
    int n_init, n_x[10], n_sync[NST];
    int left_stage;
};
HD constexpr int xpre(bool isa, int m) { return m == m_flag(isa) ? 1 : (isa && m == m_chk(isa)) ? 2 : 0; }
HD constexpr int xop(bool isa, int m) {
    if (m == M_WPF) return 1;
    if (!isa && m >= m_chk(isa) && m < m_chk(isa) + 16) return 1;
    if (!isa && m == M_PUBB) return 1;
    if (isa && m >= M_PUBA + 4 && m < M_PUBA + 6) return 1;
    if (m == m_poll(isa) || m == m_poll(isa) - 1) return 1;
    return 0;
}
HD constexpr Sched make_sched(bool isa) {
    Sched r{};
    int uk[32] = {}, ui[32] = {}, ua[32] = {}, us[32] = {}; int nu = 0;
    auto add = [&](int k, int i, int a, int sp) { uk[nu] = k; ui[nu] = i; ua[nu] = a; us[nu] = sp; ++nu; };
    auto mx = [](int x, int y) { return x > y ? x : y; };
    auto sync_m = [&](int s) { return sync_mb(isa, s); };
    auto xav = [&](int h) { return mx(h < 5 ? 0 : h == 5 ? 5 : 5 * (2 * (h - 5) + 1), 5 * (2 * h - 1) - LOOKA); };
    auto wfree = [&](int q) { return q < 4 ? 0 : q < 8 ? sync_m(q - 3) : q < 10 ? sync_m(4) : sync_m(q - 5); };
    auto wav = [&](int q) { return mx(wfree(q), sync_m(q - 1) - LOOKA); };
    add(2, 3, xav(3), 1);
    for (int q = 2; q < 5; ++q) { add(1, q, wav(q), 1); add(2, 2 * q, xav(2 * q), 1); add(2, 2 * q + 1, xav(2 * q + 1), 1); }
    add(1, 5, wav(5), 1); add(1, 6, wav(6), 1);
    for (int q = 7; q < 10; ++q) add(1, q, wfree(q), SPV);
    add(1, 10, wfree(10), SPQ0); add(1, 11, mx(wfree(11), M_Q1), SPQ);
    for (int q = 12; q < 15; ++q) add(1, q, wfree(q), SPQ);
    for (int q = 15; q < 15 + NOIN; ++q) add(1, q, wfree(q), isa ? OSPACE : OSPACEB);
    int endW[NST] = {}, endX[10] = {};
    int pos = 0;
    pos += 4; endX[0] = pos; pos += 5; endW[0] = pos; pos += 2; pos += 4; endX[1] = pos;
    pos += 5; endW[1] = pos; pos += 4; endX[2] = pos;
    r.n_init = pos - endW[0];
    int u = 0, pc = 0, nextok = 0;
    for (int m = 0; m < NMG; ++m) {
        pos += xpre(isa, m);
        r.pos[m] = pos;
        if (m % 5 == 0 && xrd_half(m / 5) >= 0) r.n_x[xrd_half(m / 5)] = endX[xrd_half(m / 5)] ? pos - endX[xrd_half(m / 5)] : -1000;
        for (int s = 0; s < 15; ++s) if (sync_m(s) == m) r.n_sync[s] = s == 14 ? 63 : endW[s + 1] ? pos - endW[s + 1] : -1000;
        if (u < nu && m >= ua[u] && m >= nextok) {
            r.kind[m] = uk[u]; r.id[m] = ui[u]; r.piece[m] = pc;
            ++pos; ++pc; nextok = m + us[u];
            int np = uk[u] == 1 ? 5 : 4;
            if (pc == np) { if (uk[u] == 1) endW[ui[u]] = pos; else endX[ui[u]] = pos; pc = 0; ++u; }
        }
        pos += xop(isa, m);
    }
    r.pos[NMG] = pos;
    r.left_stage = u < nu ? ui[u] : NST;
    return r;
}
constexpr Sched SCHED_A = make_sched(true), SCHED_B = make_sched(false);
static_assert(SCHED_A.left_stage == NST && SCHED_B.left_stage == NST, "all LDS-DMA pieces must be placed");
HD constexpr int cap63(int n) { return n > 63 ? 63 : n; }

template <int V> struct IC { static constexpr int v = V; };
template <int I, int N, class F> DI void sfor(F&& f) { if constexpr (I < N) { f(IC<I>{}); sfor<I + 1, N>(f); } }

template <bool TRANS, int FIRST> DI void mml(f32x4& d, const u32x4& x, const u32x4& w) {
    if constexpr (FIRST == 2) {
        if constexpr (TRANS) asm volatile("v_mfma_f32_16x16x32_bf16 %0, %2, %1, 1.0" : "=&v"(d) : "a"(x), "v"(w));
        else                 asm volatile("v_mfma_f32_16x16x32_bf16 %0, %1, %2, 1.0" : "=&v"(d) : "a"(x), "v"(w));
    } else if constexpr (FIRST == 1) {
        if constexpr (TRANS) asm volatile("v_mfma_f32_16x16x32_bf16 %0, %2, %1, 0" : "=&v"(d) : "a"(x), "v"(w));
        else                 asm volatile("v_mfma_f32_16x16x32_bf16 %0, %1, %2, 0" : "=&v"(d) : "a"(x), "v"(w));
    } else {
        if constexpr (TRANS) asm volatile("v_mfma_f32_16x16x32_bf16 %0, %2, %1, %0" : "+v"(d) : "a"(x), "v"(w));
        else                 asm volatile("v_mfma_f32_16x16x32_bf16 %0, %1, %2, %0" : "+v"(d) : "a"(x), "v"(w));
    }
}
DI void mv0(f32x4& d, const u32x4& a, const u32x4& b) { asm volatile("v_mfma_f32_16x16x32_bf16 %0, %1, %2, 0" : "=&v"(d) : "v"(a), "v"(b)); }
DI void mva(f32x4& d, const u32x4& a, const u32x4& b) { asm volatile("v_mfma_f32_16x16x32_bf16 %0, %1, %2, %0" : "+v"(d) : "v"(a), "v"(b)); }
DI void mvc(f32x4& d, const u32x4& a, const u32x4& b, const f32x4& c) { asm volatile("v_mfma_f32_16x16x32_bf16 %0, %1, %2, %3" : "=&v"(d) : "v"(a), "v"(b), "v"(c)); }
#define PRE_MFMA()  do { SB(); asm volatile("s_nop 1"); SB(); } while (0)
#define POST_MFMA() do { SB(); asm volatile("s_nop 7"); SB(); } while (0)

DI u32 pk2(float a, float b) { f32x2 v = {a, b}; return __builtin_bit_cast(u32, __builtin_convertvector(v, bf16x2)); }
DI u32x2 pk4(f32x4 v) { u32x2 r = {pk2(v[0], v[1]), pk2(v[2], v[3])}; return r; }
DI u32x4 cat(u32x2 a, u32x2 b) { u32x4 r = {a[0], a[1], b[0], b[1]}; return r; }
DI u32x2 trread(u32 addr) {
    return __builtin_bit_cast(u32x2, __builtin_amdgcn_ds_read_tr16_b64_v4i16((LAS s16x4*)(size_t)addr)); }
template <class T> DI T lds_ld(u32 addr) { return *(LAS T*)(size_t)addr; }
template <class T> DI void lds_st(u32 addr, T v) { *(LAS T*)(size_t)addr = v; }
DI float vmax(float a, float b) { return __builtin_elementwise_maximum(a, b); }
DI float sel4(int i, float a, float b, float c, float d) { float r = a; r = i == 1 ? b : r; r = i == 2 ? c : r; r = i == 3 ? d : r; return r; }
DI u32 umax(u32 a, u32 b) { return __builtin_elementwise_max(a, b); }
DI u32 umax4(u32 m, u32x4 v) { return umax(umax(umax(umax(m, v[0]), v[1]), v[2]), v[3]); }
DI void swap16(float& a, float& b) { auto r = __builtin_amdgcn_permlane16_swap(__float_as_uint(a), __float_as_uint(b), false, false); a = __uint_as_float(r[0]); b = __uint_as_float(r[1]); }
DI void swap32(float& a, float& b) { auto r = __builtin_amdgcn_permlane32_swap(__float_as_uint(a), __float_as_uint(b), false, false); a = __uint_as_float(r[0]); b = __uint_as_float(r[1]); }
DI rsrc_t mk_rsrc(const void* p, u32 bytes) { return __builtin_amdgcn_make_buffer_rsrc((void*)p, (short)0, (int)bytes, 0x00020000); }
DI u64 mix(u64 e) { return e * 0x9E3779B97F4A7C15ull + 0x632BE59BD9B4E019ull; }

template <int OFF> DI void ld128_sc1(u32x4& d, u32 voff, rsrc_t rs, u32 soff) {
    asm volatile("buffer_load_dwordx4 %0, %1, %2, %3 offen offset:%4 sc1" : "=v"(d) : "v"(voff), "s"(rs), "s"(soff), "i"(OFF));
}
template <int OFF> DI void rld128_sc1(u32x4& d, u32 voff, rsrc_t rs, u32 soff) {
    asm volatile("buffer_load_dwordx4 %0, %1, %2, %3 offen offset:%4 sc1" : "+v"(d) : "v"(voff), "s"(rs), "s"(soff), "i"(OFF));
}
DI void ld64_sc1(u32x2& d, u32 voff, rsrc_t rs, u32 soff) {
    asm volatile("buffer_load_dwordx2 %0, %1, %2, %3 offen sc1" : "=v"(d) : "v"(voff), "s"(rs), "s"(soff));
}
template <int OFF> DI void st128_sc1(u32x4 v, u32 voff, rsrc_t rs, u32 soff) {
    asm volatile("buffer_store_dwordx4 %0, %1, %2, %3 offen offset:%4 sc1\n\ts_nop 1" :: "v"(v), "v"(voff), "s"(rs), "s"(soff), "i"(OFF) : "memory");
}
template <int OFF> DI void st64_sc1(u32x2 v, u32 voff, rsrc_t rs, u32 soff) {
    asm volatile("buffer_store_dwordx2 %0, %1, %2, %3 offen offset:%4 sc1" :: "v"(v), "v"(voff), "s"(rs), "s"(soff), "i"(OFF) : "memory");
}
template <int LDSOFF> DI void set_m0(u32 base) { asm volatile("s_add_u32 m0, %0, %1" :: "s"(base), "i"(LDSOFF) : "scc"); }
DI void glds_go(u32 voff, rsrc_t rs, u32 soff) {
    asm volatile("buffer_load_dwordx4 %0, %1, %2 offen lds" :: "v"(voff), "s"(rs), "s"(soff)); }
template <int LDSOFF> DI void glds(u32 base, u32 voff, rsrc_t rs, u32 soff) {
    asm volatile("s_add_u32 m0, %0, %4\n\ts_nop 0\n\tbuffer_load_dwordx4 %1, %2, %3 offen lds"
                 :: "s"(base), "v"(voff), "s"(rs), "s"(soff), "i"(LDSOFF) : "scc");
}
template <int N> DI void wait_vm() { asm volatile("s_waitcnt vmcnt(%0)" :: "i"(N) : "memory"); }
DI void pin(u32x4& a) { asm volatile("" : "+v"(a)); }
DI void pin(u32x2& a) { asm volatile("" : "+v"(a)); }
DI void pin(u32& a) { asm volatile("" : "+v"(a)); }
#define PINE(v, i) do { u32 e_ = (v)[i]; pin(e_); (v)[i] = e_; } while (0)
DI bool all_eq(u32x2 v, u32x2 tag) { return __builtin_amdgcn_ballot_w64((v[0] != tag[0]) | (v[1] != tag[1])) == 0; }

DI void poll(rsrc_t ws, u32 voff, u32x2 tag) {
#pragma unroll 1
    for (int it = 0; it < (1 << 18); ++it) {
        u32x2 v;
        asm volatile("buffer_load_dwordx2 %0, %1, %2, 0 offen sc1\n\ts_waitcnt vmcnt(0)" : "=v"(v) : "v"(voff), "s"(ws) : "memory");
        if (all_eq(v, tag)) break;
        __builtin_amdgcn_s_sleep(1);
    }
}

#define TS(i) do {} while (0)

constexpr float LOG2E = 1.4426950408889634f;
DI u32 phi2(float a, float b) {
    f32x2 u = {a, b};
    f32x2 y = __builtin_elementwise_fma(u, (f32x2){LOG2E, LOG2E}, (f32x2){-LOG2E, -LOG2E});
    f32x2 r = {vmax(u[0], __builtin_amdgcn_fmed3f(__builtin_amdgcn_exp2f(y[0]), 0.f, 1.f)),
               vmax(u[1], __builtin_amdgcn_fmed3f(__builtin_amdgcn_exp2f(y[1]), 0.f, 1.f))};
    r = r + 1e-6f;
    return pk2(r[0], r[1]);
}
DI void pinf(float& a) { asm volatile("" : "+v"(a)); }

template <bool ISA>
DI void body(const rsrc_t rsX, const rsrc_t rsW, const u16* __restrict__ wog, u16* __restrict__ outg,
             char* wsp, int t, int g, int w, u32 lds0, const u32 (&vx)[4], const u32 (&vw)[5], int rot, u32 cpf, u64 ts0) {
    constexpr const Sched& SC = ISA ? SCHED_A : SCHED_B;
    constexpr int M_FLAG = m_flag(ISA), M_POLL = m_poll(ISA), M_CHK = m_chk(ISA);
    const int tid = threadIdx.x;
    const int lane = tid & 63;
    const int gam = lane >> 4, c16 = lane & 15, hc = c16 >> 2;
    const int b = ISA ? 0 : 1;
    const int wg = (b * 16 + t) * 8 + g;
    if constexpr (ISA) asm volatile(".Lfk_beg_a:"); else asm volatile(".Lfk_beg_b:");
    u32 rck[5];
#pragma unroll
    for (int j = 0; j < 5; ++j) { int c = j + rot; c = c >= 5 ? c - 5 : c; rck[j] = (u32)c; }
    const u32 ldsw = lds0 + (u32)w * 5120u;
    const u32 ldsx = lds0 + XST_OFF + (u32)w * 20480u;
    auto soffX = [&](auto H) { return rck[H.v >> 1] * 256u + (u32)(H.v & 1) * 128u; };
    auto issueX = [&](auto H) { const u32 so = soffX(H); sfor<0, 4>([&](auto J) { glds<(H.v % 5) * 4096 + J.v * 1024>(ldsx, vx[J.v], rsX, so); }); };
    const u32 wbase = (u32)w * (32u * 3840u) + (u32)g * 160u;
    auto soffW = [&](auto S) { return rck[st_chunk(S.v)] * (128u * 3840u) + (u32)(st_type(S.v) * 1280) + wbase; };
    auto issueW = [&](auto S) { const u32 so = soffW(S); sfor<0, 5>([&](auto I) { glds<st_slot(S.v) * SLOT_B + I.v * 1024>(ldsw, vw[I.v], rsW, so); }); };
    const rsrc_t rsWo = mk_rsrc(wog, DM * DM * 2);
    const rsrc_t rsWS = mk_rsrc(wsp, WS_BYTES);
    u16* const outp = outg + (size_t)(t * 128 + w * 32) * (2 * DM) + b * DM + g * 80;
    const rsrc_t rsO = mk_rsrc(outp, 31 * 2 * DM * 2 + 160);
    u32x2 ep; u32 touch;
    {
        const u32 tv = lane == 0 ? (ISA ? (u32)(128 * (1 + t)) : 0u) : lane == 1 ? (ISA ? (u32)(128 * t) : 0u)
                     : lane == 2 ? WS_O + (u32)((b * 16 + t) * 4 + w) * OBLK + 40960u : lane == 3 ? WS_P1 + (u32)wg * 8192u + 8064u : lane == 4 ? WS_O - 128u : OOB;
        ld64_sc1(ep, tv, rsWS, 0u);
        const rsrc_t rsT = mk_rsrc((w & 1) ? (const void*)wog : (const void*)outp, (w & 1) ? DM * DM * 2 : 160);
        asm volatile("buffer_load_dword %0, %1, %2, 0 offen" : "=v"(touch) : "v"((u32)lane * 65536u), "s"(rsT));
    }
    SB();

    u32 vo[5], vo4[5];
#pragma unroll
    for (int i = 0; i < 5; ++i) {
        int c = i * 64 + lane;
        int rl = c / 10, ch = c - rl * 10;
        int k = rl ^ ((rl >> 1) & 4);
        int ga = k >> 3, e = k & 7;
        vo[i] = (u32)((20 * (e >> 2) + 4 * ga + (e & 3)) * 1280 + ch * 16);
        vo4[i] = (u32)((80 * ((g + 2 * w + (e >> 2)) & 7) + 20 * ga + 16 + (e & 3)) * 1280 + ch * 16);
        asm volatile("" : "+v"(vo[i]), "+v"(vo4[i]));
    }
    auto soffO = [&](auto S) {
        constexpr int ck = st_chunk(S.v);
        const u32 row = ck < 4 ? 80u * (u32)((g + 2 * ck + (w >> 1)) & 7) + 40u * (u32)(w & 1) : 0u;
        return row * 1280u + (u32)g * 160u;
    };

    u32 rb[2][2][2];
    {
        int q = (lane >> 2) & 3, p = lane & 3;
#pragma unroll
        for (int tt = 0; tt < 2; ++tt) {
            int row = 8 * gam + 4 * (tt ^ (gam & 1)) + q;
            rb[0][tt][0] = lds0 + (u32)(row * 160 + 8 * p);
            rb[0][tt][1] = lds0 + (u32)(row * 160 + 40 * p + 32);
            rb[1][tt][0] = rb[0][tt][0] + 3 * SLOT_B;
            rb[1][tt][1] = rb[0][tt][1] + 3 * SLOT_B;
            asm volatile("" : "+v"(rb[0][tt][0]), "+v"(rb[0][tt][1]), "+v"(rb[1][tt][0]), "+v"(rb[1][tt][1]));
        }
    }
    u32x2 Bh[2][5][2];
    auto readB = [&](auto N, auto F, auto T) {
        constexpr int n = N.v, s = n >> 2, kk = n & 3, slot = st_slot(s), hf = slot / 3, f = F.v;
        constexpr int off = (slot % 3) * SLOT_B + kk * 5120;
        Bh[n & 1][f][T.v] = f < 4 ? trread(rb[hf][T.v][0] + off + 40 * f) : trread(rb[hf][T.v][1] + off);
    };
    auto readBall = [&](auto N) { sfor<0, 5>([&](auto F) { readB(N, F, IC<0>{}); readB(N, F, IC<1>{}); SB(); }); };
    u32 rx[2];
#pragma unroll
    for (int kk = 0; kk < 2; ++kk) { rx[kk] = ldsx + (u32)(c16 * 128 + (((4 * kk + gam) ^ (c16 >> 1)) * 16)); asm volatile("" : "+v"(rx[kk])); }
    u32x4 xf[2][20];
    auto readX = [&](auto H, auto J) {
        constexpr int h = H.v, p = J.v & 1, kk = J.v >> 1;
        xf[p][h * 2 + kk] = lds_ld<u32x4>(rx[kk] + (h % 5) * 4096 + p * 2048);
    };

    f32x4 acc[2][5], acv[2][5];
    const u32 xch = lds0 + XCH_OFF;
    u32 vl = (u32)lane * 16u;
    const u32 myP1 = WS_P1 + (u32)wg * 8192u;
    const u32 obase = WS_O + (u32)((b * 16 + t) * 4 + w) * OBLK;
    u32 vflag = lane < 8 ? vl : OOB;
    u32 vh0 = (ISA && w == 3 && lane >= 32) ? OOB : vl;
    u32 vh1 = (ISA && w == 3) ? OOB : vl;
    u32 f1src = ISA ? WS_F1 + (u32)((t > 0 ? wg - 8 : wg) * 4 + w) * 128u : WS_F1 + (u32)((128 + g) * 4 + w) * 128u + (u32)c16 * 4096u;
    u32 f2src = WS_F2 + (u32)(((b * 16 + t) * 8 + (lane & 7)) * 4 + w) * 128u;
    u32 vst0 = xch + (u32)(w * 5120 + c16 * 320 + gam * 8), vst1 = xch + (u32)(w * 5120 + c16 * 320 + gam * 40 + 32);
    u32 vout[5];
#pragma unroll
    for (int i = 0; i < 5; ++i) { int c = i * 64 + lane; int rl = c / 10; vout[i] = (u32)(rl * 2560 + (c - rl * 10) * 16); asm volatile("" : "+v"(vout[i])); }
    u32x4 on = {0x3F803F80u, 0x3F803F80u, 0x3F803F80u, 0x3F803F80u};
    asm volatile("" : "+v"(vl), "+v"(vflag), "+v"(vh0), "+v"(vh1), "+v"(f1src), "+v"(f2src), "+v"(vst0), "+v"(vst1), "+v"(on));
    u32x2 tag, tagp; u32x4 tag4;
    u32 wpf, vwpf = lane < 50 ? (u32)((((t & 3) * 8 + g) * 4 + w) * 50 + lane) * 128u : OOB;
    asm volatile("" : "+v"(vwpf));

    f32x4 cown, cprv; u32x2 mown, mprv;
    u32 kwr, vwr, krd, vrd;
    u32x4 S1;
    u32 a1m[4], a2m[4], a2n[4], a3m[4], l8, zrd, vgl, swr;
    if constexpr (ISA) {
        const bool hasprev = (w > 0) || (t > 0);
        bool vo_[4], vp_[4];
#pragma unroll
        for (int j = 0; j < 4; ++j) {
            int kk = 4 * gam + j;
            vo_[j] = (kk <= c16) && (kk >= c16 - 7);
            vp_[j] = (kk >= c16 + 9) && hasprev;
            cown[j] = vo_[j] ? 0.f : -1e30f;
            cprv[j] = vp_[j] ? 0.f : -1e30f;
        }
        mown[0] = (vo_[0] ? 0xFFFFu : 0u) | (vo_[1] ? 0xFFFF0000u : 0u);
        mown[1] = (vo_[2] ? 0xFFFFu : 0u) | (vo_[3] ? 0xFFFF0000u : 0u);
        mprv[0] = (vp_[0] ? 0xFFFFu : 0u) | (vp_[1] ? 0xFFFF0000u : 0u);
        mprv[1] = (vp_[2] ? 0xFFFFu : 0u) | (vp_[3] ? 0xFFFF0000u : 0u);
        kwr = xch + (c16 >= 8 ? (u32)(w + 1) * 8192u : 0u) + (u32)(gam * 8 + (c16 & 7)) * 16u;
        vwr = xch + (gam >= 2 ? (u32)(w + 1) * 8192u : 0u) + 4096u + (u32)(lane & 31) * 8u;
        krd = xch + (u32)w * 8192u + (u32)(gam * 8 + (c16 & 7)) * 16u;
        vrd = xch + (u32)w * 8192u + 4096u + (u32)(lane & 31) * 8u;
        asm volatile("" : "+v"(cown), "+v"(cprv), "+v"(mown), "+v"(mprv), "+v"(kwr), "+v"(vwr), "+v"(krd), "+v"(vrd));
    } else {
        const u32 one = ((gam & 1) == (c16 >> 3)) ? ((c16 & 1) ? 0x3F800000u : 0x00003F80u) : 0u;
#pragma unroll
        for (int d = 0; d < 4; ++d) S1[d] = ((c16 >> 1) & 3) == d ? one : 0u;
        vgl = (u32)(lane & 31) * 16u + (u32)(lane >> 5) * (8u * 8u * 8192u);
        swr = xch + 32768u + (u32)(2 * w + (gam & 1)) * 512u + (u32)((gam >> 1) * 16 + c16) * 8u;
        l8 = xch + 32768u + (u32)lane * 8u; zrd = xch + 32768u + (u32)(6 * 512) + (u32)gam * 128u;
        const u32 zb = xch + 32768u + 4096u;
#pragma unroll
        for (int h = 0; h < 4; ++h) {
            a1m[h] = gam == h ? l8 + 4 * 512 : zb;
            a2m[h] = hc == h ? l8 + 5 * 512 : zb;
            a2n[h] = (hc == h && gam == h) ? l8 + 7 * 512 : zb;
            a3m[h] = gam == h ? xch + 32768u + (u32)(7 * 512 + h * 128 + ((h + 1) & 3) * 32) : zb;
            asm volatile("" : "+v"(a1m[h]), "+v"(a2m[h]), "+v"(a2n[h]), "+v"(a3m[h]));
        }
        asm volatile("" : "+v"(S1), "+v"(l8), "+v"(zrd), "+v"(vgl), "+v"(swr));
    }

    u32x4 Kown[2][4], Kprv[2][4], Vop[2][5];
    u32x4 PK[5], VV[5], pub;
    f32x4 G[4], d1[4], d2[4], zz[4], d3, z4, rs4[2][4];
    float ph[2];
    u32 gwr = xch + (u32)w * 8192u + vl, grd = xch + (u32)w * 2048u + vl;
    asm volatile("" : "+v"(gwr), "+v"(grd));
    u32x4 hcp[2];
    u32x2 flg, fl2;
    u32x4 gl[16];

    auto filler = [&](auto M) {
        constexpr int m = M.v;
        if constexpr (m == 60) {
            pin(ep); pin(touch); pin(cpf);
            u64 a = mix(((u64)(u32)__builtin_amdgcn_readlane(ep[1], 0) << 32) | (u32)__builtin_amdgcn_readlane(ep[0], 0));
            u64 c = mix(((u64)(u32)__builtin_amdgcn_readlane(ep[1], 1) << 32) | (u32)__builtin_amdgcn_readlane(ep[0], 1));
            tag = (u32x2){(u32)a, (u32)(a >> 32)}; tagp = (u32x2){(u32)c, (u32)(c >> 32)};
            tag4 = cat(tag, tag); pin(tag4);
            asm volatile("" : "+s"(tagp));
        }
        if constexpr (m == M_WPF) asm volatile("buffer_load_dword %0, %1, %2, 0 offen" : "=v"(wpf) : "v"(vwpf), "s"(rsWo));
        if constexpr (ISA) {
            if constexpr (m >= 142 && m < 182) {
                constexpr int i = (m - 142) / 5, d = (m - 142) % 5, p = i >> 2, h = i & 3, f = d < 2 ? h : 4;
                if constexpr (d < 4) { u32 v_ = pk2(acc[p][f][(d & 1) * 2], acc[p][f][(d & 1) * 2 + 1]); pin(v_); Kown[p][h][d] = v_; if constexpr (d == 3) asm volatile("" : "+v"(acc[p][4])); }
                else lds_st(kwr + i * 512, Kown[p][h]);
            }
            if constexpr (m == M_PUBA) {
                hcp[0] = lds_ld<u32x4>(xch + 4 * 8192 + (u32)w * 2048u + vl);
                hcp[1] = lds_ld<u32x4>(xch + 4 * 8192 + (u32)w * 2048u + vl + 1024);
            }
            if constexpr (m == M_PUBA + 4) st128_sc1<0>(hcp[0], vh0, rsWS, myP1 + (u32)w * 2048u);
            if constexpr (m == M_PUBA + 5) st128_sc1<1024>(hcp[1], vh1, rsWS, myP1 + (u32)w * 2048u);
            if constexpr (m >= 295) {
                constexpr int i = m - 295;
                if constexpr (i < 4) { Kprv[i >> 1][(i & 1) * 2] = lds_ld<u32x4>(krd + 2 * i * 512); Kprv[i >> 1][(i & 1) * 2 + 1] = lds_ld<u32x4>(krd + (2 * i + 1) * 512); }
#pragma unroll
                for (int q = 2 * i; q < 2 * i + 2; ++q) { u32x2 v = lds_ld<u32x2>(vrd + q * 256); Vop[q / 5][q % 5][2] = v[0]; Vop[q / 5][q % 5][3] = v[1]; }
            }
        } else {
            if constexpr (m >= 100 && m < 172) {
                constexpr int i = m - 100, pr = i / 9, st = i % 9, f = 3 + pr / 4, d = pr % 4, e = st & 1;
                const float u = acc[d >> 1][f][(d & 1) * 2 + e];
                if constexpr (st < 2) { ph[e] = __builtin_fmaf(u, LOG2E, -LOG2E); pinf(ph[e]); }
                else if constexpr (st < 4) { ph[e] = __builtin_amdgcn_fmed3f(__builtin_amdgcn_exp2f(ph[e]), 0.f, 1.f); pinf(ph[e]); }
                else if constexpr (st < 6) { ph[e] = vmax(u, ph[e]); pinf(ph[e]); }
                else if constexpr (st < 8) { ph[e] = ph[e] + 1e-6f; pinf(ph[e]); }
                else { u32 v_ = pk2(ph[0], ph[1]); pin(v_); PK[f][d] = v_; }
            }
            if constexpr (m >= 174 && m < 178) mv0(zz[m - 174], PK[m - 174], on);
            if constexpr (m == 178) mv0(z4, PK[4], on);
            if constexpr (m >= 181 && m < 193) {
                constexpr int j = (m - 181) / 3, h = 1 + (m - 181) % 3;
                float v_ = hc == h ? zz[h][j] : zz[0][j]; pinf(v_); zz[0][j] = v_;
            }
            if constexpr (m == 193) lds_st(gwr + 6 * 1024, zz[0]);
            if constexpr (m == 194) lds_st(l8 + 4096u, (u32x2){0u, 0u});
            if constexpr (m >= 200 && m < 205) {
                constexpr int f = m == 200 ? 0 : m == 201 ? 1 : m == 202 ? 4 : m == 203 ? 2 : 3;
                VV[f] = cat(pk4(acv[0][f]), pk4(acv[1][f])); pin(VV[f]);
            }
            if constexpr (m == 202) { mv0(d1[0], PK[4], VV[0]); mv0(G[0], PK[0], VV[0]); }
            if constexpr (m == 203) { mv0(d1[1], PK[4], VV[1]); mv0(G[1], PK[1], VV[1]); }
            if constexpr (m == 204) { mv0(d2[0], PK[0], VV[4]); mv0(d2[1], PK[1], VV[4]); }
            if constexpr (m == 205) { mv0(d2[2], PK[2], VV[4]); mv0(d2[3], PK[3], VV[4]); }
            if constexpr (m == 206) { mv0(d3, PK[4], VV[4]); mv0(d1[2], PK[4], VV[2]); }
            if constexpr (m == 207) { mv0(d1[3], PK[4], VV[3]); mv0(G[2], PK[2], VV[2]); }
            if constexpr (m == 208) mv0(G[3], PK[3], VV[3]);
            if constexpr (m == 205 || m == 206) lds_st(gwr + (m - 205) * 1024, G[m - 205]);
            if constexpr (m >= 208 && m < 211) {
#pragma unroll
                for (int j = 0; j < 4; ++j) { float v_ = hc == m - 207 ? d2[m - 207][j] : d2[0][j]; pinf(v_); d2[0][j] = v_; }
            }
            if constexpr (m == 209) {
#pragma unroll
                for (int j = 0; j < 4; ++j) { float v_ = gam == hc ? d3[j] : z4[j]; pinf(v_); d3[j] = v_; }
            }
            if constexpr (m == 210 || m == 211) lds_st(gwr + (m - 208) * 1024, G[m - 208]);
            if constexpr (m >= 211 && m < 214) {
#pragma unroll
                for (int j = 0; j < 4; ++j) { float v_ = gam == m - 210 ? d1[m - 210][j] : d1[0][j]; pinf(v_); d1[0][j] = v_; }
            }
            if constexpr (m == 212) lds_st(gwr + 5 * 1024, d2[0]);
            if constexpr (m == 213) lds_st(gwr + 7 * 1024, d3);
            if constexpr (m == 214) lds_st(gwr + 4 * 1024, d1[0]);
            if constexpr (m == 215 || m == 216) {
#pragma unroll
                for (int q = 0; q < 4; ++q) rs4[m - 215][q] = lds_ld<f32x4>(grd + (m - 215) * 1024 + q * 8192);
            }
            if constexpr (m >= 219 && m < 223) {
                constexpr int i = (m - 219) >> 1, j0 = ((m - 219) & 1) * 2;
                float v0 = rs4[i][0][j0] + rs4[i][1][j0]; pinf(v0); float v1 = rs4[i][0][j0 + 1] + rs4[i][1][j0 + 1]; pinf(v1);
                float v2 = rs4[i][2][j0] + rs4[i][3][j0]; pinf(v2); float v3 = rs4[i][2][j0 + 1] + rs4[i][3][j0 + 1]; pinf(v3);
                v0 += v2; pinf(v0); v1 += v3; pinf(v1);
                u32 v_ = pk2(v0, v1); pin(v_); pub[m - 219] = v_;
            }
            if constexpr (m == M_PUBB) st128_sc1<0>(pub, vl, rsWS, myP1 + (u32)w * 1024u);
            if constexpr (m >= M_CHK && m < M_CHK + 16) ld128_sc1<((m - M_CHK) & 1) * 512>(gl[m - M_CHK], vgl, rsWS, WS_P1 + (u32)((16 + ((((m - M_CHK) >> 1) + 2 * t) & 7)) * 8 + g) * 8192u + (u32)w * 1024u);
        }
        if constexpr (m == M_POLL - 1) ld64_sc1(fl2, f2src, rsWS, 0u);
        if constexpr (m == M_POLL) ld64_sc1(flg, f1src, rsWS, 0u);
    };

    auto sync = [&](auto S, auto LG) {
        constexpr int s = S.v;
        static_assert(SC.n_sync[s] >= 0, "W piece not issued yet");
        constexpr int xc = sync_mb(ISA, s) % 20 == 15 ? xrd_half(sync_mb(ISA, s) / 5) : -1;
        constexpr int nw = (xc >= 0 && SC.n_x[xc < 0 ? 0 : xc] < SC.n_sync[s]) ? SC.n_x[xc < 0 ? 0 : xc] : SC.n_sync[s];
        SB();
        if constexpr (LG.v) asm volatile("s_waitcnt vmcnt(%0) lgkmcnt(0)\n\ts_barrier" :: "i"(cap63(nw)) : "memory");
        else asm volatile("s_waitcnt vmcnt(%0)\n\ts_barrier" :: "i"(cap63(nw)) : "memory");
        SB();
    };
    auto group = [&](auto M) {
        constexpr int m = M.v, n = m / 5, f = m % 5, s = n >> 2, kk = n & 3, ty = st_type(s), ck = st_chunk(s);
        constexpr bool trans = ty == 0 || (ISA && ty == 1);
        constexpr int first = (ck == 0 && kk == 0) ? ((!ISA && ty != 2) ? 2 : 1) : 0;
        constexpr bool pre = (n + 1) % 20 != 0 || n == (ISA ? 19 : 39);
        constexpr int xc = xrd_half(n);
        static_assert(xc < 0 || SC.n_x[xc < 0 ? 0 : xc] >= 0, "x piece not issued yet");
        if constexpr (m == sync_mb(ISA, s)) sync(IC<s>{}, IC<(ISA ? s == 14 : s == 10)>{});
        else if constexpr (f == 0 && xc >= 0) { wait_vm<cap63(SC.n_x[xc < 0 ? 0 : xc])>(); SB(); }
        constexpr int vk = SC.kind[m], vi = SC.id[m], vp = SC.piece[m];
        const u32x4 Bw = cat(Bh[n & 1][f][0], Bh[n & 1][f][1]);
        if constexpr (vk == 1) set_m0<st_slot(vi) * SLOT_B + vp * 1024>(ldsw);
        if constexpr (vk == 2) set_m0<(vi % 5) * 4096 + vp * 1024>(ldsx);
        mml<trans, first>(ty == 2 ? acv[0][f] : acc[0][f], xf[0][ck * 4 + kk], Bw);
        SB();
        if constexpr (pre) readB(IC<n + 1>{}, IC<f>{}, IC<0>{});
        if constexpr (vk == 1) {
            if constexpr (st_type(vi) == 3) glds_go(st_chunk(vi) < 4 ? vo[vp] : vo4[vp], rsWo, soffO(IC<vi>{}));
            else glds_go(vw[vp], rsW, soffW(IC<vi>{}));
        }
        if constexpr (vk == 2) glds_go(vx[vp], rsX, soffX(IC<vi>{}));
        if constexpr (xc >= 0 && f < 4) readX(IC<xc>{}, IC<f>{});
        filler(M);
        SB();
        mml<trans, first>(ty == 2 ? acv[1][f] : acc[1][f], xf[1][ck * 4 + kk], Bw);
        SB();
        if constexpr (pre) readB(IC<n + 1>{}, IC<f>{}, IC<1>{});
        SB();
    };

    SB();
    issueX(IC<1>{}); issueW(IC<1>{}); issueX(IC<2>{});
    SB();
    wait_vm<SC.n_init>();
    sfor<0, 4>([&](auto J) { readX(IC<0>{}, J); });
    asm volatile("s_barrier" ::: "memory");
    TS(1);
    readBall(IC<0>{});

    sfor<0, 100>(group);
    if constexpr (!ISA) {
        POST_MFMA();
        f32x4 ex[6];
#pragma unroll
        for (int i = 0; i < 6; ++i) {
            const f32x4 u = acc[i & 1][i >> 1];
            const f32x2 c2 = {LOG2E, LOG2E}, m2 = {-LOG2E, -LOG2E};
            f32x2 e0 = __builtin_elementwise_fma((f32x2){u[0], u[1]}, c2, m2), e1 = __builtin_elementwise_fma((f32x2){u[2], u[3]}, c2, m2);
            ex[i] = (f32x4){e0[0], e0[1], e1[0], e1[1]};
        }
        SB();
#pragma unroll
        for (int i = 0; i < 24; ++i) ex[i >> 2][i & 3] = __builtin_amdgcn_fmed3f(__builtin_amdgcn_exp2f(ex[i >> 2][i & 3]), 0.f, 1.f);
        SB();
#pragma unroll
        for (int i = 0; i < 24; ++i) ex[i >> 2][i & 3] = vmax(acc[(i >> 2) & 1][i >> 3][i & 3], ex[i >> 2][i & 3]);
        SB();
#pragma unroll
        for (int i = 0; i < 6; ++i) ex[i] = ex[i] + 1e-6f;
        SB();
#pragma unroll
        for (int f = 0; f < 3; ++f) { PK[f] = cat(pk4(ex[2 * f]), pk4(ex[2 * f + 1])); pin(PK[f]); }
    }
    if constexpr (!ISA) { sync(IC<4>{}, IC<0>{}); readBall(IC<20>{}); }
    sfor<100, 200>(group);
    TS(2);
    if constexpr (ISA) {
        POST_MFMA();
#pragma unroll
        for (int i = 0; i < 10; ++i) {
            u32x2 v = pk4(acv[i / 5][i % 5]);
            Vop[i / 5][i % 5][0] = v[0]; Vop[i / 5][i % 5][1] = v[1];
            lds_st(vwr + i * 256, v);
        }
        sync(IC<9>{}, IC<1>{});
        readBall(IC<40>{});
    }
    TS(3);

    sfor<200, M_FLAG>(group);
    wait_vm<SC.pos[M_FLAG] - 1 - SC.pos[ISA ? M_PUBA + 6 : M_PUBB + 1]>();
    st128_sc1<0>(tag4, vflag, rsWS, WS_F1 + (u32)(wg * 4 + w) * 128u);
    SB();
    sfor<M_FLAG, M_CHK>(group);
    TS(4);
    {
        wait_vm<SC.pos[M_CHK] - xpre(ISA, M_CHK) - SC.pos[M_POLL + 1]>();
        pin(flg); pin(fl2); pin(wpf);
        if constexpr (ISA) {
            if (t > 0 && !all_eq(flg, tagp)) poll(rsWS, f1src, tagp);
            SB();
            const u32 so = t > 0 ? myP1 - 8u * 8192u + (u32)w * 2048u : OOB;
            ld128_sc1<0>(hcp[0], vh0, rsWS, so);
            ld128_sc1<1024>(hcp[1], vh1, rsWS, so);
        } else {
            if (!all_eq(flg, tag)) poll(rsWS, f1src, tag);
        }
        SB();
    }
    TS(5);
    sfor<M_CHK, M_PARK>(group);
    if constexpr (ISA) {
        wait_vm<SC.pos[M_PARK] - SC.pos[M_CHK]>();
        pin(hcp[0]); pin(hcp[1]);
        lds_st(xch + (u32)w * 2048u + vl, hcp[0]);
        lds_st(xch + (u32)w * 2048u + vl + 1024, hcp[1]);
        SB();
    }
    sfor<M_PARK, 300>(group);
    auto wopiece = [&](auto J) {
        constexpr int st = 15 + NOIN + J.v / 5, i = J.v % 5;
        if constexpr (st < 20) { SB(); glds<st_slot(st) * SLOT_B + i * 1024>(ldsw, st_chunk(st) < 4 ? vo[i] : vo4[i], rsWo, soffO(IC<st>{})); SB(); }
    };
    static_assert(NOIN >= 3, "at most ten pieces are placed in the epilogue");
    POST_MFMA();
    TS(6);

    u32x4 of[2][20];
    u32x4 hq[8];
    auto publish = [&](auto P, u32x2 h4) {
        constexpr int p = P.v;
        st128_sc1<0>(of[p][0], vl, rsWS, obase + (u32)p * 16384u + (u32)g * 2048u);
        st128_sc1<1024>(of[p][1], vl, rsWS, obase + (u32)p * 16384u + (u32)g * 2048u);
        hq[0][2 * p] = h4[0]; hq[0][2 * p + 1] = h4[1];
        if constexpr (p == 1) st128_sc1<0>(hq[0], vl, rsWS, obase + 32768u + (u32)g * 1024u);
    };
    if constexpr (ISA) {
        u32x4 Qf[2][4];
        f32x4 so[2][4], sp[2][4];
        sfor<0, 2>([&](auto P) {
            constexpr int p = P.v;
            u32x2 q4 = pk4(acc[p][4]);
            sfor<0, 4>([&](auto H) {
                constexpr int h = H.v, i = p * 4 + h;
                Qf[p][h] = cat(pk4(acc[p][h]), (u32x2){gam == h ? q4[0] : 0u, gam == h ? q4[1] : 0u});
                SB();
                if constexpr (i % 2 == 0) wopiece(IC<i / 2>{});
                if constexpr (i > 0) {
                    mvc(so[(i - 1) >> 2][(i - 1) & 3], Kown[(i - 1) >> 2][(i - 1) & 3], Qf[(i - 1) >> 2][(i - 1) & 3], cown);
                    mvc(sp[(i - 1) >> 2][(i - 1) & 3], Kprv[(i - 1) >> 2][(i - 1) & 3], Qf[(i - 1) >> 2][(i - 1) & 3], cprv);
                    SB();
                }
            });
        });
        mvc(so[1][3], Kown[1][3], Qf[1][3], cown);
        mvc(sp[1][3], Kprv[1][3], Qf[1][3], cprv);
        SB();
        asm volatile("s_nop 2" :: "v"(cown), "v"(cprv));
        SB();
        const float cs = 0.22360679774997896f * 1.4426950408889634f;
        f32x4 oh[2][4], o4[2][4], dn[2][4];
        u32x4 Pf[2][4];
        auto pv = [&](auto P, auto I) {
            constexpr int p = P.v, h = I.v / 3, k = I.v % 3;
            SB();
            if constexpr (k == 0) mv0(dn[p][h], on, Pf[p][h]); else if constexpr (k == 1) mv0(oh[p][h], Vop[p][h], Pf[p][h]); else mv0(o4[p][h], Vop[p][4], Pf[p][h]);
            SB();
        };
        sfor<0, 2>([&](auto P) {
            constexpr int p = P.v;
            f32x4 L[4]; float mx[4];
            sfor<0, 4>([&](auto H) {
                constexpr int h = H.v;
#pragma unroll
                for (int j = 0; j < 4; ++j) L[h][j] = vmax(so[p][h][j], sp[p][h][j]);
                mx[h] = vmax(vmax(vmax(L[h][0], L[h][1]), L[h][2]), L[h][3]);
                if constexpr (p == 0 && h % 2 == 0) wopiece(IC<4 + h / 2>{});
                if constexpr (p == 1) pv(IC<0>{}, IC<h>{});
            });
            swap16(mx[0], mx[1]); swap16(mx[2], mx[3]);
            float xa = vmax(mx[0], mx[1]), xb = vmax(mx[2], mx[3]);
            if constexpr (p == 1) pv(IC<0>{}, IC<4>{});
            swap32(xa, xb);
            float z0 = vmax(xa, xb) * -cs, z1 = z0;
            if constexpr (p == 1) pv(IC<0>{}, IC<5>{});
            swap32(z0, z1);
            float m0 = z0, m1 = z0, m2 = z1, m3 = z1;
            if constexpr (p == 1) pv(IC<0>{}, IC<6>{});
            swap16(m0, m1); swap16(m2, m3);
            const float mc[4] = {m0, m1, m2, m3};
            if constexpr (p == 1) pv(IC<0>{}, IC<7>{});
            sfor<0, 4>([&](auto H) {
                constexpr int h = H.v;
                u32 p0 = pk2(__builtin_amdgcn_exp2f(__builtin_fmaf(L[h][0], cs, mc[h])), __builtin_amdgcn_exp2f(__builtin_fmaf(L[h][1], cs, mc[h])));
                u32 p1 = pk2(__builtin_amdgcn_exp2f(__builtin_fmaf(L[h][2], cs, mc[h])), __builtin_amdgcn_exp2f(__builtin_fmaf(L[h][3], cs, mc[h])));
                Pf[p][h] = (u32x4){p0 & mown[0], p1 & mown[1], p0 & mprv[0], p1 & mprv[1]};
                if constexpr (p == 0) wopiece(IC<6 + h>{});
                if constexpr (p == 1) pv(IC<0>{}, IC<8 + h>{});
            });
            SB();
        });
        sfor<0, 2>([&](auto P) {
            constexpr int p = P.v;
            if constexpr (p == 1) POST_MFMA();
            float r[4]; u32x2 o2[4];
            sfor<0, 4>([&](auto H) {
                constexpr int h = H.v;
                if constexpr (p == 0) { pv(IC<1>{}, IC<2 * h>{}); }
                asm volatile("" : "+v"(dn[p][h]));
                r[h] = __builtin_amdgcn_rcpf(dn[p][h][0]);
                f32x4 t4 = {oh[p][h][0] * r[h], oh[p][h][1] * r[h], oh[p][h][2] * r[h], oh[p][h][3] * r[h]};
                if constexpr (p == 0) { pv(IC<1>{}, IC<2 * h + 1>{}); }
                o2[h] = pk4(t4);
            });
            f32x4 s4;
            sfor<0, 4>([&](auto J) {
                if constexpr (p == 0) { pv(IC<1>{}, IC<8 + J.v>{}); }
                s4[J.v] = sel4(gam, o4[p][0][J.v], o4[p][1][J.v], o4[p][2][J.v], o4[p][3][J.v]);
            });
            float r4 = sel4(gam, r[0], r[1], r[2], r[3]);
            of[p][0] = cat(o2[0], o2[1]); of[p][1] = cat(o2[2], o2[3]);
            publish(P, pk4(s4 * r4));
            SB();
        });
    } else {
        u32x4 Qf[2][4];
        f32x4 sm[2], ex[2][5];
        u32x4 A1[4], A2[4], A3[4];
        f32x4 nh[2][4], n4[2], dn[2][4];
        auto nd = [&](auto P, auto I) {
            constexpr int p = P.v, h = I.v / 3, k = I.v % 3;
            SB();
            if constexpr (k == 0) mv0(dn[p][h], A3[h], Qf[p][h]); else if constexpr (k == 1) mv0(nh[p][h], A1[h], Qf[p][h]);
            else if constexpr (h == 0) mv0(n4[p], A2[0], Qf[p][0]); else mva(n4[p], A2[h], Qf[p][h]);
            SB();
        };
        auto red = [&](auto J) {
            constexpr int j = J.v;
            SB();
            if constexpr (!(j & 3)) { wait_vm<cap63(SC.pos[NMG] - SC.pos[M_CHK + j + 4 < NMG ? M_CHK + j + 4 : NMG])>(); pin(gl[j]); pin(gl[j + 1]); pin(gl[j + 2]); pin(gl[j + 3]); SB(); }
            if constexpr (j < 2) mv0(sm[j & 1], S1, gl[j]); else mva(sm[j & 1], S1, gl[j]);
            SB();
        };
#pragma unroll
        for (int i = 0; i < 10; ++i) {
            const f32x4 u = acc[i / 5][i % 5];
            const f32x2 c2 = {LOG2E, LOG2E}, m2 = {-LOG2E, -LOG2E};
            f32x2 e0 = __builtin_elementwise_fma((f32x2){u[0], u[1]}, c2, m2), e1 = __builtin_elementwise_fma((f32x2){u[2], u[3]}, c2, m2);
            ex[i / 5][i % 5] = (f32x4){e0[0], e0[1], e1[0], e1[1]};
        }
        SB();
#pragma unroll
        for (int i = 0; i < 10; ++i)
#pragma unroll
            for (int j = 0; j < 4; ++j) ex[i / 5][i % 5][j] = __builtin_amdgcn_fmed3f(__builtin_amdgcn_exp2f(ex[i / 5][i % 5][j]), 0.f, 1.f);
        SB();
        sfor<0, 20>([&](auto I) {
            constexpr int i = I.v >> 1, j = (I.v & 1) * 2;
            if constexpr (I.v < 16) red(I);
            float v0 = vmax(acc[i / 5][i % 5][j], ex[i / 5][i % 5][j]); pinf(v0); ex[i / 5][i % 5][j] = v0;
            float v1 = vmax(acc[i / 5][i % 5][j + 1], ex[i / 5][i % 5][j + 1]); pinf(v1); ex[i / 5][i % 5][j + 1] = v1;
            SB();
        });
        lds_st(swr, pk4(sm[0]));
        lds_st(swr + 256u, pk4(sm[1]));
        SB();
#pragma unroll
        for (int i = 0; i < 10; ++i) ex[i / 5][i % 5] = ex[i / 5][i % 5] + 1e-6f;
        SB();
        asm volatile("s_waitcnt lgkmcnt(0)\n\ts_barrier" ::: "memory");
#pragma unroll
        for (int h = 0; h < 4; ++h) {
            A3[h] = cat(lds_ld<u32x2>(zrd + h * 32), lds_ld<u32x2>(a3m[h]));
            A1[h] = cat(lds_ld<u32x2>(l8 + h * 512), lds_ld<u32x2>(a1m[h]));
            A2[h] = cat(lds_ld<u32x2>(a2m[h]), lds_ld<u32x2>(a2n[h]));
            SB();
            Qf[0][h] = cat(pk4(ex[0][h]), pk4(ex[0][4])); pin(Qf[0][h]);
            asm volatile("" : "+v"(ex[0][4]));
            SB();
        }
        sfor<0, 12>([&](auto I) {
            nd(IC<0>{}, I);
            if constexpr (I.v < 8) {
                constexpr int h = I.v >> 1, hi = I.v & 1;
                u32x2 v_ = pk4(ex[1][hi ? 4 : h]); pin(v_); Qf[1][h][2 * hi] = v_[0]; Qf[1][h][2 * hi + 1] = v_[1];
                if constexpr (hi) asm volatile("" : "+v"(ex[1][4]));
            } else wopiece(IC<I.v - 8>{});
        });
        float r[2][4];
        sfor<0, 12>([&](auto I) {
            nd(IC<1>{}, I);
            if constexpr (I.v < 4) { asm volatile("" : "+v"(dn[0][I.v])); r[0][I.v] = dn[0][I.v][0] + 1e-6f; pinf(r[0][I.v]); }
            else if constexpr (I.v < 8) { r[0][I.v - 4] = __builtin_amdgcn_rcpf(r[0][I.v - 4]); pinf(r[0][I.v - 4]); }
            else wopiece(IC<I.v - 4>{});
        });
        sfor<0, 2>([&](auto P) {
            constexpr int p = P.v;
            if constexpr (p == 1) {
                sfor<8, 10>(wopiece);
#pragma unroll
                for (int h = 0; h < 4; ++h) { asm volatile("" : "+v"(dn[1][h])); r[1][h] = dn[1][h][0] + 1e-6f; }
                SB();
#pragma unroll
                for (int h = 0; h < 4; ++h) r[1][h] = __builtin_amdgcn_rcpf(r[1][h]);
                SB();
            }
            float r4 = sel4(gam, r[p][0], r[p][1], r[p][2], r[p][3]);
            SB();
#pragma unroll
            for (int h = 0; h < 4; ++h) nh[p][h] = nh[p][h] * r[p][h];
            n4[p] = n4[p] * r4;
            SB();
            of[p][0] = cat(pk4(nh[p][0]), pk4(nh[p][1])); of[p][1] = cat(pk4(nh[p][2]), pk4(nh[p][3]));
            publish(P, pk4(n4[p]));
            SB();
        });
    }
    TS(7);
    wait_vm<0>();
    st128_sc1<0>(tag4, vflag, rsWS, WS_F2 + (u32)(wg * 4 + w) * 128u);
    asm volatile("s_barrier" ::: "memory");
    TS(8);
#define OAHEAD 6
    auto oload = [&](auto K, auto P) {
        constexpr int k = K.v;
        if constexpr (k < 16) ld128_sc1<(k & 1) * 1024>(of[P.v][k], vl, rsWS, obase + (u32)P.v * 16384u + (u32)((g + k / 2) & 7) * 2048u);
        else if constexpr (k > 16 || P.v == 1) ld128_sc1<0>(hq[2 * (k - 16) + P.v], vl, rsWS, obase + 32768u + (u32)((g + 2 * (k - 16) + P.v) & 7) * 1024u);
    };
    auto kstep = [&](auto K) {
        constexpr int k = K.v, n = 60 + k;
        sfor<0, 5>([&](auto F) {
            constexpr int f = F.v;
            const u32x4 Bw = cat(Bh[n & 1][f][0], Bh[n & 1][f][1]);
            if constexpr (k == 0) mv0(acc[0][f], Bw, of[0][k]); else mva(acc[0][f], Bw, of[0][k]);
            SB();
            if constexpr (k < 19) readB(IC<n + 1>{}, F, IC<0>{});
            if constexpr (k >= 2 && k + OAHEAD < 20 && (f == 1 || f == 3)) oload(IC<k + OAHEAD>{}, IC<(f >> 1)>{});
            SB();
            if constexpr (k == 0) mv0(acc[1][f], Bw, of[1][k]); else mva(acc[1][f], Bw, of[1][k]);
            SB();
            if constexpr (k < 19) readB(IC<n + 1>{}, F, IC<1>{});
            SB();
        });
    };
    readBall(IC<60>{});
    sfor<0, 2>(kstep);
    poll(rsWS, f2src, tag);
    TS(9);
    sfor<2, 2 + OAHEAD>([&](auto K) { oload(K, IC<0>{}); oload(K, IC<1>{}); });
    SB();
    sfor<2, 20>([&](auto K) {
        constexpr int k = K.v, last = k + OAHEAD - 1 < 19 ? k + OAHEAD - 1 : 19;
        wait_vm<2 * (last - k) - ((k < 16 && last >= 16) ? 1 : 0)>();
        if constexpr (k < 16) { pin(of[0][k]); pin(of[1][k]); }
        else {
            u32x4& a = hq[2 * (k - 16)]; u32x4& c = hq[2 * (k - 16) + 1];
            pin(a); pin(c);
            of[0][k] = (u32x4){a[0], a[1], c[0], c[1]}; of[1][k] = (u32x4){a[2], a[3], c[2], c[3]};
            pin(of[0][k]); pin(of[1][k]);
            PRE_MFMA();
        }
        SB();
        kstep(K);
    });
    POST_MFMA();
    TS(10);
#pragma unroll
    for (int p = 0; p < 2; ++p) {
#pragma unroll
        for (int f = 0; f < 4; ++f) lds_st(vst0 + (p * 160 + f * 40), pk4(acc[p][f]));
        lds_st(vst1 + p * 160, pk4(acc[p][4]));
    }
#pragma unroll
    for (int i = 0; i < 5; ++i)
        __builtin_amdgcn_raw_buffer_store_b128(lds_ld<u32x4>(xch + (u32)w * 5120u + vl + i * 1024), rsO, vout[i], 0, OAUX);
    if (g == 0 && w == 0) {
        if constexpr (ISA) {
            if (t > 0) st128_sc1<0>(cat(tagp, tagp), vflag, rsWS, WS_EPOCH + (u32)(128 * t));
            if (t == 15) st128_sc1<0>(tag4, vflag, rsWS, WS_EPOCH + (u32)(128 * 16));
        } else {
            if (t == 0) st128_sc1<0>(tag4, vflag, rsWS, WS_EPOCH);
        }
    }
    TS(11);
    if constexpr (ISA) asm volatile(".Lfk_end_a:"); else asm volatile(".Lfk_end_b:");
}

__global__ __launch_bounds__(256, 1) void fused_kernel(const u16* __restrict__ xa, const u16* __restrict__ wa, const u16* __restrict__ woa,
                                                       const u16* __restrict__ xb, const u16* __restrict__ wb, const u16* __restrict__ wob,
                                                       u16* __restrict__ out, char* ws) {
    __shared__ __attribute__((aligned(16))) char smem[LDS_B];
    const int x = blockIdx.x & 7, idx = blockIdx.x >> 3;
    const int t = (x & 3) * 4 + (idx >> 3), g = idx & 7;
#define ASEL 0xF0
    const bool isa = (ASEL >> x) & 1;
    typedef __attribute__((ext_vector_type(2))) u64 u64x2;
    u64x2 kxw, kow; u64 kwo;
    {
        const u64 ka = (u64)__builtin_amdgcn_kernarg_segment_ptr();
        const u32 ko = isa ? 0u : 24u, ko2 = ko + 16u;
        asm volatile("s_load_dwordx4 %0, %3, %4\n\ts_load_dwordx2 %1, %3, %5\n\ts_load_dwordx4 %2, %3, 0x30"
                     : "=&s"(kxw), "=&s"(kwo), "=&s"(kow) : "s"(ka), "s"(ko), "s"(ko2));
    }
    u64 ts0 = 0;
    const int lane = threadIdx.x & 63;
    const int w = __builtin_amdgcn_readfirstlane(threadIdx.x >> 6);
    const u32 lds0 = (u32)(size_t)(LAS char*)smem;
    u32 cpf;
    {
        u64 pc; u32 oa, la, ob, lb;
        asm volatile("s_getpc_b64 %0\n.Lfk_pc:\n\ts_mov_b32 %1, .Lfk_beg_a-.Lfk_pc\n\ts_mov_b32 %2, .Lfk_end_a-.Lfk_pc\n\ts_mov_b32 %3, .Lfk_beg_b-.Lfk_pc\n\ts_mov_b32 %4, .Lfk_end_b-.Lfk_pc"
                     : "=s"(pc), "=s"(oa), "=s"(la), "=s"(ob), "=s"(lb));
        u32 off = isa ? oa : ob, len = isa ? la : lb;
        len = len > 131072u ? 0u : len;
        const rsrc_t rsC = mk_rsrc((const void*)pc, len);
        asm volatile("buffer_load_dword %0, %1, %2, %3 offen" : "=v"(cpf) : "v"(lane < 16 ? (u32)((g & 3) + 4 * (lane + 16 * w)) * 128u : OOB), "s"(rsC), "s"(off));
    }
    u32 vx[4], vw[5];
#pragma unroll
    for (int j = 0; j < 4; ++j) { int i = 8 * (j & 1) + (lane >> 3); vx[j] = (u32)((2 * i + (j >> 1)) * 1280 + (((lane & 7) ^ (i >> 1)) * 16)); pin(vx[j]); }
#pragma unroll
    for (int i = 0; i < 5; ++i) {
        int c = i * 64 + lane, rl = c / 10, ch = c - rl * 10;
        vw[i] = (u32)((rl ^ ((rl >> 1) & 4)) * 3840 + ch * 16); pin(vw[i]);
    }
    const int rot = (t + g) % 5;
    SB();
    asm volatile("s_waitcnt lgkmcnt(0)" : "+s"(kxw), "+s"(kwo), "+s"(kow), "+s"(ts0));
    const u16* xp = (const u16*)kxw[0]; const u16* wp = (const u16*)kxw[1]; const u16* wop = (const u16*)kwo;
    out = (u16*)kow[0]; ws = (char*)kow[1];
    const rsrc_t rsX = mk_rsrc(xp + (size_t)(t * 128 + w * 32) * DM, 32 * DM * 2);
    const rsrc_t rsW = mk_rsrc(wp, DM * 3 * DM * 2);
    sfor<0, 4>([&](auto J) { glds<XST_OFF + J.v * 1024>(lds0 + (u32)w * 20480u, vx[J.v], rsX, (u32)rot * 256u); });
    sfor<0, 5>([&](auto I) { glds<I.v * 1024>(lds0 + (u32)w * 5120u, vw[I.v], rsW, (u32)rot * (128u * 3840u) + 1280u + (u32)w * (32u * 3840u) + (u32)g * 160u); });
    SB();
    if (isa) body<true>(rsX, rsW, wop, out, ws, t, g, w, lds0, vx, vw, rot, cpf, ts0);
    else     body<false>(rsX, rsW, wop, out, ws, t, g, w, lds0, vx, vw, rot, cpf, ts0);
}

extern "C" void kernel_launch(void* const* d_in, const int* in_sizes, int n_in,
                              void* d_out, int out_size, void* d_ws, size_t ws_size,
                              hipStream_t stream) {
    fused_kernel<<<256, 256, 0, stream>>>((const u16*)d_in[0], (const u16*)d_in[1], (const u16*)d_in[2],
                                          (const u16*)d_in[3], (const u16*)d_in[4], (const u16*)d_in[5],
                                          (u16*)d_out, (char*)d_ws);
}
```

```cpp
#include <hip/hip_runtime.h>
#include <stdint.h>

typedef unsigned short u16;
typedef unsigned int u32;
typedef unsigned long long u64;
typedef __attribute__((ext_vector_type(4))) short s16x4;
typedef __attribute__((ext_vector_type(2))) __bf16 bf16x2;
typedef __attribute__((ext_vector_type(4))) float f32x4;
typedef __attribute__((ext_vector_type(2))) float f32x2;
typedef __attribute__((ext_vector_type(4))) u32 u32x4;
typedef __attribute__((ext_vector_type(2))) u32 u32x2;
typedef __amdgpu_buffer_rsrc_t rsrc_t;
#define LAS __attribute__((address_space(3)))
#define DI __device__ __forceinline__
#define HD __host__ __device__
#define SB() __builtin_amdgcn_sched_barrier(0)

constexpr int OAUX = 2;

constexpr int DM = 640;
constexpr int SLOT_B = 20480;
constexpr int XST_OFF = 4 * SLOT_B;
constexpr int XCH_OFF = 6 * SLOT_B;
constexpr int LDS_B = 163840;
constexpr u32 OOB = 0x7FFFFF00u;

constexpr u32 WS_EPOCH = 0;
constexpr u32 WS_F1 = 4096;
constexpr u32 WS_F2 = WS_F1 + 256 * 4 * 128;
constexpr u32 WS_P1 = WS_F2 + 256 * 4 * 128;
constexpr u32 WS_O = WS_P1 + 256 * 8192;
constexpr u32 OBLK = 2 * 20480 + 128;
constexpr u32 WS_BYTES = WS_O + 2 * 16 * 4 * OBLK;

constexpr int NST = 20;
HD constexpr int st_type(int s)  { return s < 5 ? 1 : s < 10 ? 2 : s < 15 ? 0 : 3; }
HD constexpr int st_chunk(int s) { return s % 5; }
HD constexpr int st_slot(int s)  { return s < 8 ? s % 4 : (s - 4) % 6; }
HD constexpr int sync_mb(bool isa, int s) { return (isa ? s == 9 : s == 4) ? 20 * s + 20 : 20 * s + 15; }
HD constexpr int xrd_half(int n) { return (n & 1) && n < 19 ? (n + 1) / 2 : -1; }

constexpr int NMG = 300;
constexpr int M_WPF = 141;
constexpr int M_PUBA = 200;
constexpr int MA_FLAG = 228;
constexpr int MA_POLL = 250;
constexpr int MA_CHK = 272;
constexpr int MB_FLAG = 232;
constexpr int MB_POLL = 262;
constexpr int MB_CHK = 282;
constexpr int M_PUBB = 210;
HD constexpr int m_flag(bool isa) { return isa ? MA_FLAG : MB_FLAG; }
HD constexpr int m_poll(bool isa) { return isa ? MA_POLL : MB_POLL; }
HD constexpr int m_chk(bool isa) { return isa ? MA_CHK : MB_CHK; }
constexpr int M_PARK = 293;
constexpr int LOOKA = 50;
constexpr int OSPACE = 4;
constexpr int OSPACEB = 3;
constexpr int SPV = 2;
constexpr int SPQ0 = 2;
constexpr int M_Q1 = 180;
constexpr int SPQ = 3;
constexpr int NOIN = 3;
struct Sched {
    int kind[NMG], id[NMG], piece[NMG];
    int pos[NMG + 1];
    int n_init, n_x[10], n_sync[NST];
    int left_stage;
};
HD constexpr int xpre(bool isa, int m) { return m == m_flag(isa) ? 1 : (isa && m == m_chk(isa)) ? 2 : 0; }
HD constexpr int xop(bool isa, int m) {
    if (m == M_WPF) return 1;
    if (!isa && m >= m_chk(isa) && m < m_chk(isa) + 16) return 1;
    if (!isa && m == M_PUBB) return 1;
    if (isa && m >= M_PUBA + 4 && m < M_PUBA + 6) return 1;
    if (m == m_poll(isa) || m == m_poll(isa) - 1) return 1;
    return 0;
}
HD constexpr Sched make_sched(bool isa) {
    Sched r{};
    int uk[32] = {}, ui[32] = {}, ua[32] = {}, us[32] = {}; int nu = 0;
    auto add = [&](int k, int i, int a, int sp) { uk[nu] = k; ui[nu] = i; ua[nu] = a; us[nu] = sp; ++nu; };
    auto mx = [](int x, int y) { return x > y ? x : y; };
    auto sync_m = [&](int s) { return sync_mb(isa, s); };
    auto xav = [&](int h) { return mx(h < 5 ? 0 : h == 5 ? 5 : 5 * (2 * (h - 5) + 1), 5 * (2 * h - 1) - LOOKA); };
    auto wfree = [&](int q) { return q < 4 ? 0 : q < 8 ? sync_m(q - 3) : q < 10 ? sync_m(4) : sync_m(q - 5); };
    auto wav = [&](int q) { return mx(wfree(q), sync_m(q - 1) - LOOKA); };
    add(2, 3, xav(3), 1);
    for (int q = 2; q < 5; ++q) { add(1, q, wav(q), 1); add(2, 2 * q, xav(2 * q), 1); add(2, 2 * q + 1, xav(2 * q + 1), 1); }
    add(1, 5, wav(5), 1); add(1, 6, wav(6), 1);
    for (int q = 7; q < 10; ++q) add(1, q, wfree(q), SPV);
    add(1, 10, wfree(10), SPQ0); add(1, 11, mx(wfree(11), M_Q1), SPQ);
    for (int q = 12; q < 15; ++q) add(1, q, wfree(q), SPQ);
    for (int q = 15; q < 15 + NOIN; ++q) add(1, q, wfree(q), isa ? OSPACE : OSPACEB);
    int endW[NST] = {}, endX[10] = {};
    int pos = 0;
    pos += 4; endX[0] = pos; pos += 5; endW[0] = pos; pos += 2; pos += 4; endX[1] = pos;
    pos += 5; endW[1] = pos; pos += 4; endX[2] = pos;
    r.n_init = pos - endW[0];
    int u = 0, pc = 0, nextok = 0;
    for (int m = 0; m < NMG; ++m) {
        pos += xpre(isa, m);
        r.pos[m] = pos;
        if (m % 5 == 0 && xrd_half(m / 5) >= 0) r.n_x[xrd_half(m / 5)] = endX[xrd_half(m / 5)] ? pos - endX[xrd_half(m / 5)] : -1000;
        for (int s = 0; s < 15; ++s) if (sync_m(s) == m) r.n_sync[s] = s == 14 ? 63 : endW[s + 1] ? pos - endW[s + 1] : -1000;
        if (u < nu && m >= ua[u] && m >= nextok) {
            r.kind[m] = uk[u]; r.id[m] = ui[u]; r.piece[m] = pc;
            ++pos; ++pc; nextok = m + us[u];
            int np = uk[u] == 1 ? 5 : 4;
            if (pc == np) { if (uk[u] == 1) endW[ui[u]] = pos; else endX[ui[u]] = pos; pc = 0; ++u; }
        }
        pos += xop(isa, m);
    }
    r.pos[NMG] = pos;
    r.left_stage = u < nu ? ui[u] : NST;
    return r;
}
constexpr Sched SCHED_A = make_sched(true), SCHED_B = make_sched(false);
static_assert(SCHED_A.left_stage == NST && SCHED_B.left_stage == NST, "all LDS-DMA pieces must be placed");
HD constexpr int cap63(int n) { return n > 63 ? 63 : n; }

template <int V> struct IC { static constexpr int v = V; };
template <int I, int N, class F> DI void sfor(F&& f) { if constexpr (I < N) { f(IC<I>{}); sfor<I + 1, N>(f); } }

template <bool TRANS, int FIRST> DI void mml(f32x4& d, const u32x4& x, const u32x4& w) {
    if constexpr (FIRST == 2) {
        if constexpr (TRANS) asm volatile("v_mfma_f32_16x16x32_bf16 %0, %2, %1, 1.0" : "=&v"(d) : "a"(x), "v"(w));
        else                 asm volatile("v_mfma_f32_16x16x32_bf16 %0, %1, %2, 1.0" : "=&v"(d) : "a"(x), "v"(w));
    } else if constexpr (FIRST == 1) {
        if constexpr (TRANS) asm volatile("v_mfma_f32_16x16x32_bf16 %0, %2, %1, 0" : "=&v"(d) : "a"(x), "v"(w));
        else                 asm volatile("v_mfma_f32_16x16x32_bf16 %0, %1, %2, 0" : "=&v"(d) : "a"(x), "v"(w));
    } else {
        if constexpr (TRANS) asm volatile("v_mfma_f32_16x16x32_bf16 %0, %2, %1, %0" : "+v"(d) : "a"(x), "v"(w));
        else                 asm volatile("v_mfma_f32_16x16x32_bf16 %0, %1, %2, %0" : "+v"(d) : "a"(x), "v"(w));
    }
}
DI void mv0(f32x4& d, const u32x4& a, const u32x4& b) { asm volatile("v_mfma_f32_16x16x32_bf16 %0, %1, %2, 0" : "=&v"(d) : "v"(a), "v"(b)); }
DI void mva(f32x4& d, const u32x4& a, const u32x4& b) { asm volatile("v_mfma_f32_16x16x32_bf16 %0, %1, %2, %0" : "+v"(d) : "v"(a), "v"(b)); }
DI void mvc(f32x4& d, const u32x4& a, const u32x4& b, const f32x4& c) { asm volatile("v_mfma_f32_16x16x32_bf16 %0, %1, %2, %3" : "=&v"(d) : "v"(a), "v"(b), "v"(c)); }
#define PRE_MFMA()  do { SB(); asm volatile("s_nop 1"); SB(); } while (0)
#define POST_MFMA() do { SB(); asm volatile("s_nop 7"); SB(); } while (0)

DI u32 pk2(float a, float b) { f32x2 v = {a, b}; return __builtin_bit_cast(u32, __builtin_convertvector(v, bf16x2)); }
DI u32x2 pk4(f32x4 v) { u32x2 r = {pk2(v[0], v[1]), pk2(v[2], v[3])}; return r; }
DI u32x4 cat(u32x2 a, u32x2 b) { u32x4 r = {a[0], a[1], b[0], b[1]}; return r; }
DI u32x2 trread(u32 addr) {
    return __builtin_bit_cast(u32x2, __builtin_amdgcn_ds_read_tr16_b64_v4i16((LAS s16x4*)(size_t)addr)); }
template <class T> DI T lds_ld(u32 addr) { return *(LAS T*)(size_t)addr; }
template <class T> DI void lds_st(u32 addr, T v) { *(LAS T*)(size_t)addr = v; }
DI float vmax(float a, float b) { return __builtin_elementwise_maximum(a, b); }
DI float sel4(int i, float a, float b, float c, float d) { float r = a; r = i == 1 ? b : r; r = i == 2 ? c : r; r = i == 3 ? d : r; return r; }
DI void swap16(float& a, float& b) { auto r = __builtin_amdgcn_permlane16_swap(__float_as_uint(a), __float_as_uint(b), false, false); a = __uint_as_float(r[0]); b = __uint_as_float(r[1]); }
DI void swap32(float& a, float& b) { auto r = __builtin_amdgcn_permlane32_swap(__float_as_uint(a), __float_as_uint(b), false, false); a = __uint_as_float(r[0]); b = __uint_as_float(r[1]); }
DI rsrc_t mk_rsrc(const void* p, u32 bytes) { return __builtin_amdgcn_make_buffer_rsrc((void*)p, (short)0, (int)bytes, 0x00020000); }
DI u64 mix(u64 e) { return e * 0x9E3779B97F4A7C15ull + 0x632BE59BD9B4E019ull; }

template <int OFF> DI void ld128_sc1(u32x4& d, u32 voff, rsrc_t rs, u32 soff) {
    asm volatile("buffer_load_dwordx4 %0, %1, %2, %3 offen offset:%4 sc1" : "=v"(d) : "v"(voff), "s"(rs), "s"(soff), "i"(OFF));
}
DI void ld64_sc1(u32x2& d, u32 voff, rsrc_t rs, u32 soff) {
    asm volatile("buffer_load_dwordx2 %0, %1, %2, %3 offen sc1" : "=v"(d) : "v"(voff), "s"(rs), "s"(soff));
}
template <int OFF> DI void st128_sc1(u32x4 v, u32 voff, rsrc_t rs, u32 soff) {
    asm volatile("buffer_store_dwordx4 %0, %1, %2, %3 offen offset:%4 sc1\n\ts_nop 1" :: "v"(v), "v"(voff), "s"(rs), "s"(soff), "i"(OFF) : "memory");
}
template <int LDSOFF> DI void set_m0(u32 base) { asm volatile("s_add_u32 m0, %0, %1" :: "s"(base), "i"(LDSOFF) : "scc"); }
DI void glds_go(u32 voff, rsrc_t rs, u32 soff) {
    asm volatile("buffer_load_dwordx4 %0, %1, %2 offen lds" :: "v"(voff), "s"(rs), "s"(soff)); }
template <int LDSOFF> DI void glds(u32 base, u32 voff, rsrc_t rs, u32 soff) {
    asm volatile("s_add_u32 m0, %0, %4\n\ts_nop 0\n\tbuffer_load_dwordx4 %1, %2, %3 offen lds"
                 :: "s"(base), "v"(voff), "s"(rs), "s"(soff), "i"(LDSOFF) : "scc");
}
template <int N> DI void wait_vm() { asm volatile("s_waitcnt vmcnt(%0)" :: "i"(N) : "memory"); }
DI void pin(u32x4& a) { asm volatile("" : "+v"(a)); }
DI void pin(u32x2& a) { asm volatile("" : "+v"(a)); }
DI void pin(u32& a) { asm volatile("" : "+v"(a)); }
DI bool all_eq(u32x2 v, u32x2 tag) { return __builtin_amdgcn_ballot_w64((v[0] != tag[0]) | (v[1] != tag[1])) == 0; }

DI void poll(rsrc_t ws, u32 voff, u32x2 tag) {
#pragma unroll 1
    for (int it = 0; it < (1 << 18); ++it) {
        u32x2 v;
        asm volatile("buffer_load_dwordx2 %0, %1, %2, 0 offen sc1\n\ts_waitcnt vmcnt(0)" : "=v"(v) : "v"(voff), "s"(ws) : "memory");
        if (all_eq(v, tag)) break;
        __builtin_amdgcn_s_sleep(1);
    }
}


constexpr float LOG2E = 1.4426950408889634f;
DI void pinf(float& a) { asm volatile("" : "+v"(a)); }

template <bool ISA>
DI void body(const rsrc_t rsX, const rsrc_t rsW, const u16* __restrict__ wog, u16* __restrict__ outg,
             char* wsp, int t, int g, int w, u32 lds0, const u32 (&vx)[4], const u32 (&vw)[5], int rot, u32 cpf) {
    constexpr const Sched& SC = ISA ? SCHED_A : SCHED_B;
    constexpr int M_FLAG = m_flag(ISA), M_POLL = m_poll(ISA), M_CHK = m_chk(ISA);
    const int tid = threadIdx.x;
    const int lane = tid & 63;
    const int gam = lane >> 4, c16 = lane & 15, hc = c16 >> 2;
    const int b = ISA ? 0 : 1;
    const int wg = (b * 16 + t) * 8 + g;
    if constexpr (ISA) asm volatile(".Lfk_beg_a:"); else asm volatile(".Lfk_beg_b:");
    u32 rck[5];
#pragma unroll
    for (int j = 0; j < 5; ++j) { int c = j + rot; c = c >= 5 ? c - 5 : c; rck[j] = (u32)c; }
    const u32 ldsw = lds0 + (u32)w * 5120u;
    const u32 ldsx = lds0 + XST_OFF + (u32)w * 20480u;
    auto soffX = [&](auto H) { return rck[H.v >> 1] * 256u + (u32)(H.v & 1) * 128u; };
    auto issueX = [&](auto H) { const u32 so = soffX(H); sfor<0, 4>([&](auto J) { glds<(H.v % 5) * 4096 + J.v * 1024>(ldsx, vx[J.v], rsX, so); }); };
    const u32 wbase = (u32)w * (32u * 3840u) + (u32)g * 160u;
    auto soffW = [&](auto S) { return rck[st_chunk(S.v)] * (128u * 3840u) + (u32)(st_type(S.v) * 1280) + wbase; };
    auto issueW = [&](auto S) { const u32 so = soffW(S); sfor<0, 5>([&](auto I) { glds<st_slot(S.v) * SLOT_B + I.v * 1024>(ldsw, vw[I.v], rsW, so); }); };
    const rsrc_t rsWo = mk_rsrc(wog, DM * DM * 2);
    const rsrc_t rsWS = mk_rsrc(wsp, WS_BYTES);
    u16* const outp = outg + (size_t)(t * 128 + w * 32) * (2 * DM) + b * DM + g * 80;
    const rsrc_t rsO = mk_rsrc(outp, 31 * 2 * DM * 2 + 160);
    u32x2 ep; u32 touch;
    {
        u32 tv = OOB;
        asm volatile("v_writelane_b32 %0, %1, 0\n\tv_writelane_b32 %0, %2, 1\n\tv_writelane_b32 %0, %3, 2\n\tv_writelane_b32 %0, %4, 3\n\tv_writelane_b32 %0, %5, 4"
                     : "+v"(tv) : "s"(ISA ? (u32)(128 * (1 + t)) : 0u), "s"(ISA ? (u32)(128 * t) : 0u), "s"(WS_O + (u32)((b * 16 + t) * 4 + w) * OBLK + 40960u),
                       "s"(WS_P1 + (u32)wg * 8192u + 8064u), "s"(WS_O - 128u));
        ld64_sc1(ep, tv, rsWS, 0u);
        const rsrc_t rsT = mk_rsrc((w & 1) ? (const void*)wog : (const void*)outp, (w & 1) ? DM * DM * 2 : 160);
        asm volatile("buffer_load_dword %0, %1, %2, 0 offen" : "=v"(touch) : "v"((u32)lane * 65536u), "s"(rsT));
    }
    SB();

    u32 vo[5], vo4[5];
#pragma unroll
    for (int i = 0; i < 5; ++i) {
        int c = i * 64 + lane;
        int rl = c / 10, ch = c - rl * 10;
        int k = rl ^ ((rl >> 1) & 4);
        int ga = k >> 3, e = k & 7;
        vo[i] = (u32)((20 * (e >> 2) + 4 * ga + (e & 3)) * 1280 + ch * 16);
        vo4[i] = (u32)((80 * ((g + 2 * w + (e >> 2)) & 7) + 20 * ga + 16 + (e & 3)) * 1280 + ch * 16);
        asm volatile("" : "+v"(vo[i]), "+v"(vo4[i]));
    }
    auto soffO = [&](auto S) {
        constexpr int ck = st_chunk(S.v);
        const u32 row = ck < 4 ? 80u * (u32)((g + 2 * ck + (w >> 1)) & 7) + 40u * (u32)(w & 1) : 0u;
        return row * 1280u + (u32)g * 160u;
    };

    u32 rb[2][2][2];
    {
        int q = (lane >> 2) & 3, p = lane & 3;
#pragma unroll
        for (int tt = 0; tt < 2; ++tt) {
            int row = 8 * gam + 4 * (tt ^ (gam & 1)) + q;
            rb[0][tt][0] = lds0 + (u32)(row * 160 + 8 * p);
            rb[0][tt][1] = lds0 + (u32)(row * 160 + 40 * p + 32);
            rb[1][tt][0] = rb[0][tt][0] + 3 * SLOT_B;
            rb[1][tt][1] = rb[0][tt][1] + 3 * SLOT_B;
            asm volatile("" : "+v"(rb[0][tt][0]), "+v"(rb[0][tt][1]), "+v"(rb[1][tt][0]), "+v"(rb[1][tt][1]));
        }
    }
    u32x2 Bh[2][5][2];
    auto readB = [&](auto N, auto F, auto T) {
        constexpr int n = N.v, s = n >> 2, kk = n & 3, slot = st_slot(s), hf = slot / 3, f = F.v;
        constexpr int off = (slot % 3) * SLOT_B + kk * 5120;
        Bh[n & 1][f][T.v] = f < 4 ? trread(rb[hf][T.v][0] + off + 40 * f) : trread(rb[hf][T.v][1] + off);
    };
    auto readBall = [&](auto N) { sfor<0, 5>([&](auto F) { readB(N, F, IC<0>{}); readB(N, F, IC<1>{}); SB(); }); };
    u32 rx[2];
#pragma unroll
    for (int kk = 0; kk < 2; ++kk) { rx[kk] = ldsx + (u32)(c16 * 128 + (((4 * kk + gam) ^ (c16 >> 1)) * 16)); asm volatile("" : "+v"(rx[kk])); }
    u32x4 xf[2][20];
    auto readX = [&](auto H, auto J) {
        constexpr int h = H.v, p = J.v & 1, kk = J.v >> 1;
        xf[p][h * 2 + kk] = lds_ld<u32x4>(rx[kk] + (h % 5) * 4096 + p * 2048);
    };

    f32x4 acc[2][5], acv[2][5];
    const u32 xch = lds0 + XCH_OFF;
    u32 vl = (u32)lane * 16u;
    const u32 myP1 = WS_P1 + (u32)wg * 8192u;
    const u32 obase = WS_O + (u32)((b * 16 + t) * 4 + w) * OBLK;
    u32 vflag = lane < 8 ? vl : OOB;
    u32 vh0 = (ISA && w == 3 && lane >= 32) ? OOB : vl;
    u32 vh1 = (ISA && w == 3) ? OOB : vl;
    u32 f1src = ISA ? WS_F1 + (u32)((t > 0 ? wg - 8 : wg) * 4 + w) * 128u : WS_F1 + (u32)((128 + g) * 4 + w) * 128u + (u32)c16 * 4096u;
    u32 f2src = WS_F2 + (u32)(((b * 16 + t) * 8 + (lane & 7)) * 4 + w) * 128u;
    u32 vst0 = xch + (u32)(w * 5632 + c16 * 176 + gam * 8), vst1 = xch + (u32)(w * 5632 + c16 * 176 + gam * 40 + 32);
    u32 vout[5], vord[5];
#pragma unroll
    for (int i = 0; i < 5; ++i) {
        int c = i * 64 + lane, rl = c / 10, ch = c - rl * 10;
        vout[i] = (u32)((2 * (rl & 15) + (rl >> 4)) * 2560 + ch * 16);
        vord[i] = xch + (u32)(w * 5632 + c * 16 + rl * 16);
        asm volatile("" : "+v"(vout[i]), "+v"(vord[i]));
    }
    u32x4 on = {0x3F803F80u, 0x3F803F80u, 0x3F803F80u, 0x3F803F80u};
    asm volatile("" : "+v"(vl), "+v"(vflag), "+v"(vh0), "+v"(vh1), "+v"(f1src), "+v"(f2src), "+v"(vst0), "+v"(vst1), "+v"(on));
    u32x2 tag, tagp; u32x4 tag4;
    u32 wpf, vwpf = lane < 50 ? (u32)((((t & 3) * 8 + g) * 4 + w) * 50 + lane) * 128u : OOB;
    asm volatile("" : "+v"(vwpf));

    f32x4 cown, cprv; u32x2 mown, mprv;
    u32 kwr, vwr, krd, vrd;
    u32x4 S1;
    u32 a1m[4], a2m[4], a2n[4], a3m[4], l8, zrd, vgl, swr;
    if constexpr (ISA) {
        const bool hasprev = (w > 0) || (t > 0);
        bool vo_[4], vp_[4];
#pragma unroll
        for (int j = 0; j < 4; ++j) {
            int kk = 4 * gam + j;
            vo_[j] = (kk <= c16) && (kk >= c16 - 7);
            vp_[j] = (kk >= c16 + 9) && hasprev;
            cown[j] = vo_[j] ? 0.f : -1e30f;
            cprv[j] = vp_[j] ? 0.f : -1e30f;
        }
        mown[0] = (vo_[0] ? 0xFFFFu : 0u) | (vo_[1] ? 0xFFFF0000u : 0u);
        mown[1] = (vo_[2] ? 0xFFFFu : 0u) | (vo_[3] ? 0xFFFF0000u : 0u);
        mprv[0] = (vp_[0] ? 0xFFFFu : 0u) | (vp_[1] ? 0xFFFF0000u : 0u);
        mprv[1] = (vp_[2] ? 0xFFFFu : 0u) | (vp_[3] ? 0xFFFF0000u : 0u);
        kwr = xch + (c16 >= 8 ? (u32)(w + 1) * 8192u : 0u) + (u32)(gam * 8 + (c16 & 7)) * 16u;
        vwr = xch + (gam >= 2 ? (u32)(w + 1) * 8192u : 0u) + 4096u + (u32)(lane & 31) * 8u;
        krd = xch + (u32)w * 8192u + (u32)(gam * 8 + (c16 & 7)) * 16u;
        vrd = xch + (u32)w * 8192u + 4096u + (u32)(lane & 31) * 8u;
        asm volatile("" : "+v"(cown), "+v"(cprv), "+v"(mown), "+v"(mprv), "+v"(kwr), "+v"(vwr), "+v"(krd), "+v"(vrd));
    } else {
        const u32 one = ((gam & 1) == (c16 >> 3)) ? ((c16 & 1) ? 0x3F800000u : 0x00003F80u) : 0u;
#pragma unroll
        for (int d = 0; d < 4; ++d) S1[d] = ((c16 >> 1) & 3) == d ? one : 0u;
        vgl = (u32)(lane & 31) * 16u + (u32)(lane >> 5) * (8u * 8u * 8192u);
        swr = xch + 32768u + (u32)(2 * w + (gam & 1)) * 512u + (u32)((gam >> 1) * 16 + c16) * 8u;
        l8 = xch + 32768u + (u32)lane * 8u; zrd = xch + 32768u + (u32)(6 * 512) + (u32)gam * 128u;
        const u32 zb = xch + 32768u + 4096u;
#pragma unroll
        for (int h = 0; h < 4; ++h) {
            a1m[h] = gam == h ? l8 + 4 * 512 : zb;
            a2m[h] = hc == h ? l8 + 5 * 512 : zb;
            a2n[h] = (hc == h && gam == h) ? l8 + 7 * 512 : zb;
            a3m[h] = gam == h ? xch + 32768u + (u32)(7 * 512 + h * 128 + ((h + 1) & 3) * 32) : zb;
            asm volatile("" : "+v"(a1m[h]), "+v"(a2m[h]), "+v"(a2n[h]), "+v"(a3m[h]));
        }
        asm volatile("" : "+v"(S1), "+v"(l8), "+v"(zrd), "+v"(vgl), "+v"(swr));
    }

    u32x4 Kown[2][4], Kprv[2][4], Vop[2][5];
    u32x4 PK[5], VV[5], pub;
    f32x4 G[4], d1[4], d2[4], zz[4], d3, z4, rs4[2][4];
    float ph[2];
    u32 gwr = xch + (u32)w * 8192u + vl, grd = xch + (u32)w * 2048u + vl;
    asm volatile("" : "+v"(gwr), "+v"(grd));
    u32x4 hcp[2];
    u32x2 flg, fl2;
    u32x4 gl[16];

    auto filler = [&](auto M) {
        constexpr int m = M.v;
        if constexpr (m == 60) {
            pin(ep); pin(touch); pin(cpf);
            u64 a = mix(((u64)(u32)__builtin_amdgcn_readlane(ep[1], 0) << 32) | (u32)__builtin_amdgcn_readlane(ep[0], 0));
            u64 c = mix(((u64)(u32)__builtin_amdgcn_readlane(ep[1], 1) << 32) | (u32)__builtin_amdgcn_readlane(ep[0], 1));
            tag = (u32x2){(u32)a, (u32)(a >> 32)}; tagp = (u32x2){(u32)c, (u32)(c >> 32)};
            tag4 = cat(tag, tag); pin(tag4);
            asm volatile("" : "+s"(tagp));
        }
        if constexpr (m == M_WPF) asm volatile("buffer_load_dword %0, %1, %2, 0 offen" : "=v"(wpf) : "v"(vwpf), "s"(rsWo));
        if constexpr (ISA) {
            if constexpr (m >= 142 && m < 182) {
                constexpr int i = (m - 142) / 5, d = (m - 142) % 5, p = i >> 2, h = i & 3, f = d < 2 ? h : 4;
                if constexpr (d < 4) { u32 v_ = pk2(acc[p][f][(d & 1) * 2], acc[p][f][(d & 1) * 2 + 1]); pin(v_); Kown[p][h][d] = v_; if constexpr (d == 3) asm volatile("" : "+v"(acc[p][4])); }
                else lds_st(kwr + i * 512, Kown[p][h]);
            }
            if constexpr (m == M_PUBA) {
                hcp[0] = lds_ld<u32x4>(xch + 4 * 8192 + (u32)w * 2048u + vl);
                hcp[1] = lds_ld<u32x4>(xch + 4 * 8192 + (u32)w * 2048u + vl + 1024);
            }
            if constexpr (m == M_PUBA + 4) st128_sc1<0>(hcp[0], vh0, rsWS, myP1 + (u32)w * 2048u);
            if constexpr (m == M_PUBA + 5) st128_sc1<1024>(hcp[1], vh1, rsWS, myP1 + (u32)w * 2048u);
            if constexpr (m >= 295) {
                constexpr int i = m - 295;
                if constexpr (i < 4) { Kprv[i >> 1][(i & 1) * 2] = lds_ld<u32x4>(krd + 2 * i * 512); Kprv[i >> 1][(i & 1) * 2 + 1] = lds_ld<u32x4>(krd + (2 * i + 1) * 512); }
#pragma unroll
                for (int q = 2 * i; q < 2 * i + 2; ++q) { u32x2 v = lds_ld<u32x2>(vrd + q * 256); Vop[q / 5][q % 5][2] = v[0]; Vop[q / 5][q % 5][3] = v[1]; }
            }
        } else {
            if constexpr (m >= 100 && m < 172) {
                constexpr int i = m - 100, pr = i / 9, st = i % 9, f = 3 + pr / 4, d = pr % 4, e = st & 1;
                const float u = acc[d >> 1][f][(d & 1) * 2 + e];
                if constexpr (st < 2) { ph[e] = __builtin_fmaf(u, LOG2E, -LOG2E); pinf(ph[e]); }
                else if constexpr (st < 4) { ph[e] = __builtin_amdgcn_fmed3f(__builtin_amdgcn_exp2f(ph[e]), 0.f, 1.f); pinf(ph[e]); }
                else if constexpr (st < 6) { ph[e] = vmax(u, ph[e]); pinf(ph[e]); }
                else if constexpr (st < 8) { ph[e] = ph[e] + 1e-6f; pinf(ph[e]); }
                else { u32 v_ = pk2(ph[0], ph[1]); pin(v_); PK[f][d] = v_; }
            }
            if constexpr (m >= 174 && m < 178) mv0(zz[m - 174], PK[m - 174], on);
            if constexpr (m == 178) mv0(z4, PK[4], on);
            if constexpr (m >= 181 && m < 193) {
                constexpr int j = (m - 181) / 3, h = 1 + (m - 181) % 3;
                float v_ = hc == h ? zz[h][j] : zz[0][j]; pinf(v_); zz[0][j] = v_;
            }
            if constexpr (m == 193) lds_st(gwr + 6 * 1024, zz[0]);
            if constexpr (m == 194) lds_st(l8 + 4096u, (u32x2){0u, 0u});
            auto packv = [&](auto F) { VV[F.v] = cat(pk4(acv[0][F.v]), pk4(acv[1][F.v])); pin(VV[F.v]); SB(); };
            auto sel = [&](f32x4 (&d)[4], int key) {
#pragma unroll
                for (int h = 1; h < 4; ++h)
#pragma unroll
                    for (int j = 0; j < 4; ++j) d[0][j] = key == h ? d[h][j] : d[0][j];
            };
            if constexpr (m == 200) { packv(IC<0>{}); packv(IC<1>{}); }
            if constexpr (m == 201) {
                mv0(d1[0], PK[4], VV[0]); SB(); packv(IC<4>{}); mv0(G[0], PK[0], VV[0]); mv0(d1[1], PK[4], VV[1]); SB(); packv(IC<2>{}); mv0(G[1], PK[1], VV[1]);
            }
            if constexpr (m == 202) { mv0(d2[0], PK[0], VV[4]); SB(); packv(IC<3>{}); mv0(d2[1], PK[1], VV[4]); mv0(d2[2], PK[2], VV[4]); mv0(d2[3], PK[3], VV[4]); }
            if constexpr (m == 203) {
                mv0(d3, PK[4], VV[4]); SB(); lds_st(gwr, G[0]); SB(); mv0(d1[2], PK[4], VV[2]); SB(); lds_st(gwr + 1024, G[1]); SB();
                mv0(d1[3], PK[4], VV[3]); mv0(G[2], PK[2], VV[2]); mv0(G[3], PK[3], VV[3]);
            }
            if constexpr (m == 204) { sel(d2, hc); lds_st(gwr + 5 * 1024, d2[0]); }
            if constexpr (m == 205) {
#pragma unroll
                for (int j = 0; j < 4; ++j) d3[j] = gam == hc ? d3[j] : z4[j];
                lds_st(gwr + 7 * 1024, d3); lds_st(gwr + 2 * 1024, G[2]); lds_st(gwr + 3 * 1024, G[3]);
                sel(d1, gam); lds_st(gwr + 4 * 1024, d1[0]);
            }
            if constexpr (m == 207) {
                asm volatile("s_waitcnt lgkmcnt(3)\n\ts_barrier" ::: "memory");
#pragma unroll
                for (int q = 0; q < 8; ++q) rs4[q >> 2][q & 3] = lds_ld<f32x4>(grd + (q >> 2) * 1024 + (q & 3) * 8192);
            }
            if constexpr (m == 209 || m == 210) {
                constexpr int i = m - 209;
#pragma unroll
                for (int j = 0; j < 4; ++j) { float v0 = rs4[i][0][j] + rs4[i][1][j]; pinf(v0); float v1 = rs4[i][2][j] + rs4[i][3][j]; pinf(v1); rs4[i][0][j] = v0; rs4[i][1][j] = v1; }
#pragma unroll
                for (int j = 0; j < 4; ++j) { float v0 = rs4[i][0][j] + rs4[i][1][j]; pinf(v0); rs4[i][0][j] = v0; }
                u32x2 v_ = pk4(rs4[i][0]); pin(v_); pub[2 * i] = v_[0]; pub[2 * i + 1] = v_[1];
            }
            if constexpr (m == M_PUBB) st128_sc1<0>(pub, vl, rsWS, myP1 + (u32)w * 1024u);
            if constexpr (m >= M_CHK && m < M_CHK + 16) {
                constexpr int j = m - M_CHK;
                ld128_sc1<(j & 1) * 512>(gl[j], vgl, rsWS, WS_P1 + (u32)((16 + (((j >> 1) + 2 * t) & 7)) * 8 + g) * 8192u + (u32)w * 1024u);
            }
        }
        if constexpr (m == M_POLL - 1) ld64_sc1(fl2, f2src, rsWS, 0u);
        if constexpr (m == M_POLL) ld64_sc1(flg, f1src, rsWS, 0u);
    };

    auto sync = [&](auto S, auto LG) {
        constexpr int s = S.v;
        static_assert(SC.n_sync[s] >= 0, "W piece not issued yet");
        constexpr int xc = sync_mb(ISA, s) % 20 == 15 ? xrd_half(sync_mb(ISA, s) / 5) : -1;
        constexpr int nw = (xc >= 0 && SC.n_x[xc < 0 ? 0 : xc] < SC.n_sync[s]) ? SC.n_x[xc < 0 ? 0 : xc] : SC.n_sync[s];
        SB();
        if constexpr (LG.v) asm volatile("s_waitcnt vmcnt(%0) lgkmcnt(0)\n\ts_barrier" :: "i"(cap63(nw)) : "memory");
        else asm volatile("s_waitcnt vmcnt(%0)\n\ts_barrier" :: "i"(cap63(nw)) : "memory");
        SB();
    };
    auto group = [&](auto M) {
        constexpr int m = M.v, n = m / 5, f = m % 5, s = n >> 2, kk = n & 3, ty = st_type(s), ck = st_chunk(s);
        constexpr bool trans = ty == 0 || (ISA && ty == 1);
        constexpr int first = (ck == 0 && kk == 0) ? ((!ISA && ty != 2) ? 2 : 1) : 0;
        constexpr bool pre = (n + 1) % 20 != 0 || n == (ISA ? 19 : 39);
        constexpr int xc = xrd_half(n);
        static_assert(xc < 0 || SC.n_x[xc < 0 ? 0 : xc] >= 0, "x piece not issued yet");
        if constexpr (m == sync_mb(ISA, s)) sync(IC<s>{}, IC<(ISA && s == 14)>{});
        else if constexpr (f == 0 && xc >= 0) { wait_vm<cap63(SC.n_x[xc < 0 ? 0 : xc])>(); SB(); }
        constexpr int vk = SC.kind[m], vi = SC.id[m], vp = SC.piece[m];
        const u32x4 Bw = cat(Bh[n & 1][f][0], Bh[n & 1][f][1]);
        if constexpr (vk == 1) set_m0<st_slot(vi) * SLOT_B + vp * 1024>(ldsw);
        if constexpr (vk == 2) set_m0<(vi % 5) * 4096 + vp * 1024>(ldsx);
        mml<trans, first>(ty == 2 ? acv[0][f] : acc[0][f], xf[0][ck * 4 + kk], Bw);
        SB();
        if constexpr (pre) readB(IC<n + 1>{}, IC<f>{}, IC<0>{});
        if constexpr (vk == 1) {
            if constexpr (st_type(vi) == 3) glds_go(st_chunk(vi) < 4 ? vo[vp] : vo4[vp], rsWo, soffO(IC<vi>{}));
            else glds_go(vw[vp], rsW, soffW(IC<vi>{}));
        }
        if constexpr (vk == 2) glds_go(vx[vp], rsX, soffX(IC<vi>{}));
        if constexpr (xc >= 0 && f < 4) readX(IC<xc>{}, IC<f>{});
        filler(M);
        SB();
        mml<trans, first>(ty == 2 ? acv[1][f] : acc[1][f], xf[1][ck * 4 + kk], Bw);
        SB();
        if constexpr (pre) readB(IC<n + 1>{}, IC<f>{}, IC<1>{});
        SB();
    };

    SB();
    issueX(IC<1>{}); issueW(IC<1>{}); issueX(IC<2>{});
    SB();
    wait_vm<SC.n_init>();
    sfor<0, 4>([&](auto J) { readX(IC<0>{}, J); });
    asm volatile("s_barrier" ::: "memory");
    readBall(IC<0>{});

    sfor<0, 100>(group);
    if constexpr (!ISA) {
        POST_MFMA();
        f32x4 ex[6];
#pragma unroll
        for (int i = 0; i < 6; ++i) {
            const f32x4 u = acc[i & 1][i >> 1];
            const f32x2 c2 = {LOG2E, LOG2E}, m2 = {-LOG2E, -LOG2E};
            f32x2 e0 = __builtin_elementwise_fma((f32x2){u[0], u[1]}, c2, m2), e1 = __builtin_elementwise_fma((f32x2){u[2], u[3]}, c2, m2);
            ex[i] = (f32x4){e0[0], e0[1], e1[0], e1[1]};
        }
        SB();
#pragma unroll
        for (int i = 0; i < 24; ++i) ex[i >> 2][i & 3] = __builtin_amdgcn_fmed3f(__builtin_amdgcn_exp2f(ex[i >> 2][i & 3]), 0.f, 1.f);
        SB();
#pragma unroll
        for (int i = 0; i < 24; ++i) ex[i >> 2][i & 3] = vmax(acc[(i >> 2) & 1][i >> 3][i & 3], ex[i >> 2][i & 3]);
        SB();
#pragma unroll
        for (int i = 0; i < 6; ++i) ex[i] = ex[i] + 1e-6f;
        SB();
#pragma unroll
        for (int f = 0; f < 3; ++f) { PK[f] = cat(pk4(ex[2 * f]), pk4(ex[2 * f + 1])); pin(PK[f]); }
    }
    if constexpr (!ISA) { sync(IC<4>{}, IC<0>{}); readBall(IC<20>{}); }
    sfor<100, 200>(group);
    if constexpr (ISA) {
        POST_MFMA();
#pragma unroll
        for (int i = 0; i < 10; ++i) {
            u32x2 v = pk4(acv[i / 5][i % 5]);
            Vop[i / 5][i % 5][0] = v[0]; Vop[i / 5][i % 5][1] = v[1];
            lds_st(vwr + i * 256, v);
        }
        sync(IC<9>{}, IC<1>{});
        readBall(IC<40>{});
    }

    sfor<200, M_FLAG>(group);
    wait_vm<SC.pos[M_FLAG] - 1 - SC.pos[ISA ? M_PUBA + 6 : M_PUBB + 1]>();
    st128_sc1<0>(tag4, vflag, rsWS, WS_F1 + (u32)(wg * 4 + w) * 128u);
    SB();
    sfor<M_FLAG, M_CHK>(group);
    {
        wait_vm<SC.pos[M_CHK] - xpre(ISA, M_CHK) - SC.pos[M_POLL + 1]>();
        pin(flg); pin(fl2); pin(wpf);
        if constexpr (ISA) {
            if (t > 0 && !all_eq(flg, tagp)) poll(rsWS, f1src, tagp);
            SB();
            const u32 so = (t > 0 ? myP1 - 8u * 8192u : myP1) + (u32)w * 2048u, tm = t > 0 ? 0u : OOB;
            ld128_sc1<0>(hcp[0], vh0 | tm, rsWS, so);
            ld128_sc1<1024>(hcp[1], vh1 | tm, rsWS, so);
        } else {
            if (!all_eq(flg, tag)) poll(rsWS, f1src, tag);
        }
        SB();
    }
    sfor<M_CHK, M_PARK>(group);
    if constexpr (ISA) {
        wait_vm<SC.pos[M_PARK] - SC.pos[M_CHK]>();
        pin(hcp[0]); pin(hcp[1]);
        lds_st(xch + (u32)w * 2048u + vl, hcp[0]);
        lds_st(xch + (u32)w * 2048u + vl + 1024, hcp[1]);
        SB();
    }
    sfor<M_PARK, 300>(group);
    POST_MFMA();

    u32x4 of[2][20];
    u32x4 hq[8];
    auto publish = [&](auto P, u32x2 h4) {
        constexpr int p = P.v;
        st128_sc1<0>(of[p][0], vl, rsWS, obase + (u32)p * 16384u + (u32)g * 2048u);
        st128_sc1<1024>(of[p][1], vl, rsWS, obase + (u32)p * 16384u + (u32)g * 2048u);
        hq[0][2 * p] = h4[0]; hq[0][2 * p + 1] = h4[1];
        if constexpr (p == 1) st128_sc1<0>(hq[0], vl, rsWS, obase + 32768u + (u32)g * 1024u);
    };
    if constexpr (ISA) {
        u32x4 Qf[2][4];
        f32x4 so[2][4], sp[2][4];
        sfor<0, 2>([&](auto P) {
            constexpr int p = P.v;
            u32x2 q4 = pk4(acc[p][4]);
            sfor<0, 4>([&](auto H) {
                constexpr int h = H.v, i = p * 4 + h;
                Qf[p][h] = cat(pk4(acc[p][h]), (u32x2){gam == h ? q4[0] : 0u, gam == h ? q4[1] : 0u});
                SB();
                if constexpr (i > 0) {
                    mvc(so[(i - 1) >> 2][(i - 1) & 3], Kown[(i - 1) >> 2][(i - 1) & 3], Qf[(i - 1) >> 2][(i - 1) & 3], cown);
                    mvc(sp[(i - 1) >> 2][(i - 1) & 3], Kprv[(i - 1) >> 2][(i - 1) & 3], Qf[(i - 1) >> 2][(i - 1) & 3], cprv);
                    SB();
                }
            });
        });
        mvc(so[1][3], Kown[1][3], Qf[1][3], cown);
        mvc(sp[1][3], Kprv[1][3], Qf[1][3], cprv);
        SB();
        asm volatile("s_nop 2" :: "v"(cown), "v"(cprv));
        SB();
        const float cs = 0.22360679774997896f * 1.4426950408889634f;
        f32x4 oh[2][4], o4[2][4], dn[2][4];
        u32x4 Pf[2][4];
        auto pv = [&](auto P, auto I) {
            constexpr int p = P.v, h = I.v / 3, k = I.v % 3;
            SB();
            if constexpr (k == 0) mv0(dn[p][h], on, Pf[p][h]); else if constexpr (k == 1) mv0(oh[p][h], Vop[p][h], Pf[p][h]); else mv0(o4[p][h], Vop[p][4], Pf[p][h]);
            SB();
        };
        sfor<0, 2>([&](auto P) {
            constexpr int p = P.v;
            f32x4 L[4]; float mx[4];
            sfor<0, 4>([&](auto H) {
                constexpr int h = H.v;
#pragma unroll
                for (int j = 0; j < 4; ++j) L[h][j] = vmax(so[p][h][j], sp[p][h][j]);
                mx[h] = vmax(vmax(vmax(L[h][0], L[h][1]), L[h][2]), L[h][3]);
                if constexpr (p == 1) pv(IC<0>{}, IC<h>{});
            });
            swap16(mx[0], mx[1]); swap16(mx[2], mx[3]);
            float xa = vmax(mx[0], mx[1]), xb = vmax(mx[2], mx[3]);
            if constexpr (p == 1) pv(IC<0>{}, IC<4>{});
            swap32(xa, xb);
            float z0 = vmax(xa, xb) * -cs, z1 = z0;
            if constexpr (p == 1) pv(IC<0>{}, IC<5>{});
            swap32(z0, z1);
            float m0 = z0, m1 = z0, m2 = z1, m3 = z1;
            if constexpr (p == 1) pv(IC<0>{}, IC<6>{});
            swap16(m0, m1); swap16(m2, m3);
            const float mc[4] = {m0, m1, m2, m3};
            if constexpr (p == 1) pv(IC<0>{}, IC<7>{});
            sfor<0, 4>([&](auto H) {
                constexpr int h = H.v;
                u32 p0 = pk2(__builtin_amdgcn_exp2f(__builtin_fmaf(L[h][0], cs, mc[h])), __builtin_amdgcn_exp2f(__builtin_fmaf(L[h][1], cs, mc[h])));
                u32 p1 = pk2(__builtin_amdgcn_exp2f(__builtin_fmaf(L[h][2], cs, mc[h])), __builtin_amdgcn_exp2f(__builtin_fmaf(L[h][3], cs, mc[h])));
                Pf[p][h] = (u32x4){p0 & mown[0], p1 & mown[1], p0 & mprv[0], p1 & mprv[1]};
                if constexpr (p == 1) pv(IC<0>{}, IC<8 + h>{});
            });
            SB();
        });
        sfor<0, 2>([&](auto P) {
            constexpr int p = P.v;
            if constexpr (p == 1) POST_MFMA();
            float r[4]; u32x2 o2[4];
            sfor<0, 4>([&](auto H) {
                constexpr int h = H.v;
                if constexpr (p == 0) { pv(IC<1>{}, IC<2 * h>{}); }
                asm volatile("" : "+v"(dn[p][h]));
                r[h] = __builtin_amdgcn_rcpf(dn[p][h][0]);
                f32x4 t4 = {oh[p][h][0] * r[h], oh[p][h][1] * r[h], oh[p][h][2] * r[h], oh[p][h][3] * r[h]};
                if constexpr (p == 0) { pv(IC<1>{}, IC<2 * h + 1>{}); }
                o2[h] = pk4(t4);
            });
            f32x4 s4;
            sfor<0, 4>([&](auto J) {
                if constexpr (p == 0) { pv(IC<1>{}, IC<8 + J.v>{}); }
                s4[J.v] = sel4(gam, o4[p][0][J.v], o4[p][1][J.v], o4[p][2][J.v], o4[p][3][J.v]);
            });
            float r4 = sel4(gam, r[0], r[1], r[2], r[3]);
            of[p][0] = cat(o2[0], o2[1]); of[p][1] = cat(o2[2], o2[3]);
            publish(P, pk4(s4 * r4));
            SB();
        });
    } else {
        u32x4 Qf[2][4];
        f32x4 sm[2], ex[2][5];
        u32x4 A1[4], A2[4], A3[4];
        f32x4 nh[2][4], n4[2], dn[2][4];
        auto nd = [&](auto P, auto I) {
            constexpr int p = P.v, h = I.v / 3, k = I.v % 3;
            SB();
            if constexpr (k == 0) mv0(dn[p][h], A3[h], Qf[p][h]); else if constexpr (k == 1) mv0(nh[p][h], A1[h], Qf[p][h]);
            else if constexpr (h == 0) mv0(n4[p], A2[0], Qf[p][0]); else mva(n4[p], A2[h], Qf[p][h]);
            SB();
        };
        auto red = [&](auto J) {
            constexpr int j = J.v;
            SB();
            if constexpr (!(j & 3)) { wait_vm<cap63(SC.pos[NMG] - SC.pos[M_CHK + j + 4])>(); pin(gl[j]); pin(gl[j + 1]); pin(gl[j + 2]); pin(gl[j + 3]); SB(); }
            if constexpr (j < 2) mv0(sm[j & 1], S1, gl[j]); else mva(sm[j & 1], S1, gl[j]);
            SB();
        };
#pragma unroll
        for (int i = 0; i < 10; ++i) {
            const f32x4 u = acc[i / 5][i % 5];
            const f32x2 c2 = {LOG2E, LOG2E}, m2 = {-LOG2E, -LOG2E};
            f32x2 e0 = __builtin_elementwise_fma((f32x2){u[0], u[1]}, c2, m2), e1 = __builtin_elementwise_fma((f32x2){u[2], u[3]}, c2, m2);
            ex[i / 5][i % 5] = (f32x4){e0[0], e0[1], e1[0], e1[1]};
        }
        SB();
#pragma unroll
        for (int i = 0; i < 10; ++i)
#pragma unroll
            for (int j = 0; j < 4; ++j) ex[i / 5][i % 5][j] = __builtin_amdgcn_fmed3f(__builtin_amdgcn_exp2f(ex[i / 5][i % 5][j]), 0.f, 1.f);
        SB();
        sfor<0, 20>([&](auto I) {
            constexpr int i = I.v >> 1, j = (I.v & 1) * 2;
            if constexpr (I.v < 16) red(I);
            float v0 = vmax(acc[i / 5][i % 5][j], ex[i / 5][i % 5][j]); pinf(v0); ex[i / 5][i % 5][j] = v0;
            float v1 = vmax(acc[i / 5][i % 5][j + 1], ex[i / 5][i % 5][j + 1]); pinf(v1); ex[i / 5][i % 5][j + 1] = v1;
            SB();
        });
        lds_st(swr, pk4(sm[0]));
        lds_st(swr + 256u, pk4(sm[1]));
        SB();
#pragma unroll
        for (int i = 0; i < 10; ++i) ex[i / 5][i % 5] = ex[i / 5][i % 5] + 1e-6f;
        SB();
        asm volatile("s_waitcnt lgkmcnt(0)\n\ts_barrier" ::: "memory");
#pragma unroll
        for (int h = 0; h < 4; ++h) {
            A3[h] = cat(lds_ld<u32x2>(zrd + h * 32), lds_ld<u32x2>(a3m[h]));
            A1[h] = cat(lds_ld<u32x2>(l8 + h * 512), lds_ld<u32x2>(a1m[h]));
            A2[h] = cat(lds_ld<u32x2>(a2m[h]), lds_ld<u32x2>(a2n[h]));
            SB();
            Qf[0][h] = cat(pk4(ex[0][h]), pk4(ex[0][4])); pin(Qf[0][h]);
            asm volatile("" : "+v"(ex[0][4]));
            SB();
        }
        sfor<0, 12>([&](auto I) {
            nd(IC<0>{}, I);
            if constexpr (I.v < 8) {
                constexpr int h = I.v >> 1, hi = I.v & 1;
                u32x2 v_ = pk4(ex[1][hi ? 4 : h]); pin(v_); Qf[1][h][2 * hi] = v_[0]; Qf[1][h][2 * hi + 1] = v_[1];
                if constexpr (hi) asm volatile("" : "+v"(ex[1][4]));
            }
        });
        float r[2][4];
        sfor<0, 12>([&](auto I) {
            nd(IC<1>{}, I);
            if constexpr (I.v < 4) { asm volatile("" : "+v"(dn[0][I.v])); r[0][I.v] = dn[0][I.v][0] + 1e-6f; pinf(r[0][I.v]); }
            else if constexpr (I.v < 8) { r[0][I.v - 4] = __builtin_amdgcn_rcpf(r[0][I.v - 4]); pinf(r[0][I.v - 4]); }
        });
        sfor<0, 2>([&](auto P) {
            constexpr int p = P.v;
            if constexpr (p == 1) {
#pragma unroll
                for (int h = 0; h < 4; ++h) { asm volatile("" : "+v"(dn[1][h])); r[1][h] = dn[1][h][0] + 1e-6f; }
                SB();
#pragma unroll
                for (int h = 0; h < 4; ++h) r[1][h] = __builtin_amdgcn_rcpf(r[1][h]);
                SB();
            }
            float r4 = sel4(gam, r[p][0], r[p][1], r[p][2], r[p][3]);
            SB();
#pragma unroll
            for (int h = 0; h < 4; ++h) nh[p][h] = nh[p][h] * r[p][h];
            n4[p] = n4[p] * r4;
            SB();
            of[p][0] = cat(pk4(nh[p][0]), pk4(nh[p][1])); of[p][1] = cat(pk4(nh[p][2]), pk4(nh[p][3]));
            publish(P, pk4(n4[p]));
            SB();
        });
    }
    wait_vm<0>();
    st128_sc1<0>(tag4, vflag, rsWS, WS_F2 + (u32)(wg * 4 + w) * 128u);
    asm volatile("s_barrier" ::: "memory");
    {
        sfor<15 + NOIN, 20>([&](auto S) {
            const u32 so = soffO(S);
            sfor<0, 5>([&](auto I) { glds<st_slot(S.v) * SLOT_B + I.v * 1024>(ldsw, st_chunk(S.v) < 4 ? vo[I.v] : vo4[I.v], rsWo, so); });
        });
        SB();
    }
constexpr int OAHEAD = 6;
    auto oload = [&](auto K, auto P) {
        constexpr int k = K.v;
        if constexpr (k < 16) ld128_sc1<(k & 1) * 1024>(of[P.v][k], vl, rsWS, obase + (u32)P.v * 16384u + (u32)((g + k / 2) & 7) * 2048u);
        else if constexpr (k > 16 || P.v == 1) ld128_sc1<0>(hq[2 * (k - 16) + P.v], vl, rsWS, obase + 32768u + (u32)((g + 2 * (k - 16) + P.v) & 7) * 1024u);
    };
    auto kstep = [&](auto K) {
        constexpr int k = K.v, n = 60 + k;
        sfor<0, 5>([&](auto F) {
            constexpr int f = F.v;
            const u32x4 Bw = cat(Bh[n & 1][f][0], Bh[n & 1][f][1]);
            if constexpr (k == 0) mv0(acc[0][f], Bw, of[0][k]); else mva(acc[0][f], Bw, of[0][k]);
            SB();
            if constexpr (k < 19) readB(IC<n + 1>{}, F, IC<0>{});
            if constexpr (k >= 2 && k + OAHEAD < 20 && (f == 1 || f == 3)) oload(IC<k + OAHEAD>{}, IC<(f >> 1)>{});
            SB();
            if constexpr (k == 0) mv0(acc[1][f], Bw, of[1][k]); else mva(acc[1][f], Bw, of[1][k]);
            SB();
            if constexpr (k < 19) readB(IC<n + 1>{}, F, IC<1>{});
            SB();
        });
    };
    readBall(IC<60>{});
    sfor<0, 2>(kstep);
    poll(rsWS, f2src, tag);
    sfor<2, 2 + OAHEAD>([&](auto K) { oload(K, IC<0>{}); oload(K, IC<1>{}); });
    SB();
    sfor<2, 20>([&](auto K) {
        constexpr int k = K.v, last = k + OAHEAD - 1 < 19 ? k + OAHEAD - 1 : 19;
        wait_vm<2 * (last - k) - ((k < 16 && last >= 16) ? 1 : 0)>();
        if constexpr (k < 16) { pin(of[0][k]); pin(of[1][k]); }
        else {
            u32x4& a = hq[2 * (k - 16)]; u32x4& c = hq[2 * (k - 16) + 1];
            pin(a); pin(c);
            of[0][k] = (u32x4){a[0], a[1], c[0], c[1]}; of[1][k] = (u32x4){a[2], a[3], c[2], c[3]};
            pin(of[0][k]); pin(of[1][k]);
            PRE_MFMA();
        }
        if constexpr (k == 4 * NOIN - 1) asm volatile("s_barrier" ::: "memory");
        SB();
        kstep(K);
    });
    POST_MFMA();
#pragma unroll
    for (int p = 0; p < 2; ++p) {
#pragma unroll
        for (int f = 0; f < 4; ++f) lds_st(vst0 + (p * 2816 + f * 40), pk4(acc[p][f]));
        lds_st(vst1 + p * 2816, pk4(acc[p][4]));
    }
#pragma unroll
    for (int i = 0; i < 5; ++i)
        __builtin_amdgcn_raw_buffer_store_b128(lds_ld<u32x4>(vord[i]), rsO, vout[i], 0, OAUX);
    if (g == 0 && w == 0) {
        if constexpr (ISA) {
            if (t > 0) st128_sc1<0>(cat(tagp, tagp), vflag, rsWS, WS_EPOCH + (u32)(128 * t));
            if (t == 15) st128_sc1<0>(tag4, vflag, rsWS, WS_EPOCH + (u32)(128 * 16));
        } else {
            if (t == 0) st128_sc1<0>(tag4, vflag, rsWS, WS_EPOCH);
        }
    }
    if constexpr (ISA) asm volatile(".Lfk_end_a:"); else asm volatile(".Lfk_end_b:");
}

__global__ __launch_bounds__(256, 1) void fused_kernel(const u16* __restrict__ xa, const u16* __restrict__ wa, const u16* __restrict__ woa,
                                                       const u16* __restrict__ xb, const u16* __restrict__ wb, const u16* __restrict__ wob,
                                                       u16* __restrict__ out, char* ws) {
    __shared__ __attribute__((aligned(16))) char smem[LDS_B];
    const int x = blockIdx.x & 7, idx = blockIdx.x >> 3;
    const int t = (x & 3) * 4 + (idx >> 3), g = idx & 7;
constexpr int ASEL = 0xF0;
    const bool isa = (ASEL >> x) & 1;
    typedef __attribute__((ext_vector_type(2))) u64 u64x2;
    u64x2 kxw, kow; u64 kwo;
    {
        const u64 ka = (u64)__builtin_amdgcn_kernarg_segment_ptr();
        const u32 ko = isa ? 0u : 24u, ko2 = ko + 16u;
        asm volatile("s_load_dwordx4 %0, %3, %4\n\ts_load_dwordx2 %1, %3, %5\n\ts_load_dwordx4 %2, %3, 0x30"
                     : "=&s"(kxw), "=&s"(kwo), "=&s"(kow) : "s"(ka), "s"(ko), "s"(ko2));
    }
    const int lane = threadIdx.x & 63;
    const int w = __builtin_amdgcn_readfirstlane(threadIdx.x >> 6);
    const u32 lds0 = (u32)(size_t)(LAS char*)smem;
    u32 cpf;
    {
        u64 pc; u32 oa, la, ob, lb;
        asm volatile("s_getpc_b64 %0\n.Lfk_pc:\n\ts_mov_b32 %1, .Lfk_beg_a-.Lfk_pc\n\ts_mov_b32 %2, .Lfk_end_a-.Lfk_pc\n\ts_mov_b32 %3, .Lfk_beg_b-.Lfk_pc\n\ts_mov_b32 %4, .Lfk_end_b-.Lfk_pc"
                     : "=s"(pc), "=s"(oa), "=s"(la), "=s"(ob), "=s"(lb));
        u32 off = isa ? oa : ob, len = isa ? la : lb;
        len = len > 131072u ? 0u : len;
        const rsrc_t rsC = mk_rsrc((const void*)pc, len);
        asm volatile("buffer_load_dword %0, %1, %2, %3 offen" : "=v"(cpf) : "v"(lane < 16 ? (u32)((g & 3) + 4 * (lane + 16 * w)) * 128u : OOB), "s"(rsC), "s"(off));
    }
    u32 vx[4], vw[5];
#pragma unroll
    for (int j = 0; j < 4; ++j) { int i = 8 * (j & 1) + (lane >> 3); vx[j] = (u32)((2 * i + (j >> 1)) * 1280 + (((lane & 7) ^ (i >> 1)) * 16)); pin(vx[j]); }
#pragma unroll
    for (int i = 0; i < 5; ++i) {
        int c = i * 64 + lane, rl = c / 10, ch = c - rl * 10;
        vw[i] = (u32)((rl ^ ((rl >> 1) & 4)) * 3840 + ch * 16); pin(vw[i]);
    }
    const int rot = (t + g) % 5;
    SB();
    asm volatile("s_waitcnt lgkmcnt(0)" : "+s"(kxw), "+s"(kwo), "+s"(kow));
    const u16* xp = (const u16*)kxw[0]; const u16* wp = (const u16*)kxw[1]; const u16* wop = (const u16*)kwo;
    out = (u16*)kow[0]; ws = (char*)kow[1];
    const rsrc_t rsX = mk_rsrc(xp + (size_t)(t * 128 + w * 32) * DM, 32 * DM * 2);
    const rsrc_t rsW = mk_rsrc(wp, DM * 3 * DM * 2);
    sfor<0, 4>([&](auto J) { glds<XST_OFF + J.v * 1024>(lds0 + (u32)w * 20480u, vx[J.v], rsX, (u32)rot * 256u); });
    sfor<0, 5>([&](auto I) { glds<I.v * 1024>(lds0 + (u32)w * 5120u, vw[I.v], rsW, (u32)rot * (128u * 3840u) + 1280u + (u32)w * (32u * 3840u) + (u32)g * 160u); });
    SB();
    if (__builtin_expect(!isa, 1)) body<false>(rsX, rsW, wop, out, ws, t, g, w, lds0, vx, vw, rot, cpf);
    else                           body<true>(rsX, rsW, wop, out, ws, t, g, w, lds0, vx, vw, rot, cpf);
}

extern "C" void kernel_launch(void* const* d_in, const int* in_sizes, int n_in,
                              void* d_out, int out_size, void* d_ws, size_t ws_size,
                              hipStream_t stream) {
    fused_kernel<<<256, 256, 0, stream>>>((const u16*)d_in[0], (const u16*)d_in[1], (const u16*)d_in[2],
                                          (const u16*)d_in[3], (const u16*)d_in[4], (const u16*)d_in[5],
                                          (u16*)d_out, (char*)d_ws);
}
```
